```python
import torch

def _softmax(x, axis=-1):
    return torch.softmax(x, dim=axis)
def _gelu(x):
    return torch.nn.functional.gelu(x, approximate="tanh")
def _silu(x):
    return torch.nn.functional.silu(x)
def _sigmoid(x):
    return torch.sigmoid(x)
def _layernorm(x, axis=-1, eps=1e-5):
    m = x.mean(dim=axis, keepdim=True)
    v = x.var(dim=axis, keepdim=True, unbiased=False)
    return (x - m) / torch.sqrt(v + eps)
def _scatter_add(tgt, idx, src, axis=0):
    return tgt.clone().index_add_(axis, idx, src)
def _segment_sum(src, seg, n):
    out = torch.zeros((n,) + src.shape[1:], dtype=src.dtype)
    return out.index_add_(0, seg, src)
def _pad(x, pw):
    flat = tuple(v for pair in reversed(pw) for v in pair)
    return torch.nn.functional.pad(x, flat)

kernel_name = 'standalone__gqa_softmax__near_ridge'

def setup_inputs():

    _w = (lambda a: (lambda t: t.to(torch.bfloat16) if t.is_floating_point() else t)(torch.as_tensor(a)))
    B, Hq, Hkv, Sq, Sk, D = 8, 32, 16, 96, 2048, 128
    return {
        "q": _w((lambda *s: torch.randn(*s, dtype=torch.bfloat16) * 0.8)(B, Hq, Sq, D)),
        "k": _w((lambda *s: torch.randn(*s, dtype=torch.bfloat16) * 0.8)(B, Hkv, Sk, D)),
        "v": _w((lambda *s: torch.randn(*s, dtype=torch.bfloat16) * 0.05)(B, Hkv, Sk, D)),
    }

def reference(q, k, v):
    B, Hq, Sq, D = q.shape
    Hkv = k.shape[1]
    rep = Hq // Hkv
    k = (lambda x, n, axis: torch.repeat_interleave(x, n, dim=axis))(k, rep, axis=1)
    v = (lambda x, n, axis: torch.repeat_interleave(x, n, dim=axis))(v, rep, axis=1)
    scale = D ** -0.5
    scores = torch.einsum("bhqd,bhkd->bhqk", q, k) * scale
    p = _softmax(scores, axis=-1)
    return torch.einsum("bhqk,bhkd->bhqd", p, v)
```

```cpp
#include <hip/hip_runtime.h>
#include <hip/hip_bf16.h>
#include <stdint.h>

namespace {
constexpr int NB = 8, HQ = 32, HKV = 16, SQ = 96, SK = 2048, D = 128;
constexpr int NWAVES = 12, NTHR = NWAVES * 64;
constexpr int TK = 128, NT = SK / TK;
constexpr int SLOT = TK * D * 2;
constexpr int LDS_K = 0, LDS_V = 2 * SLOT;
constexpr int LDS_ML = 36 * 4096;
constexpr int LDS_BYTES = LDS_ML + NWAVES * 32 * 2 * 4;
constexpr float CSC = 0.088388347648318447f * 1.4426950408889634f;
constexpr float THR = 8.0f;

typedef float f32x16 __attribute__((ext_vector_type(16)));
typedef float f32x4 __attribute__((ext_vector_type(4)));
typedef float f32x2 __attribute__((ext_vector_type(2)));
typedef short s16x8 __attribute__((ext_vector_type(8)));
typedef short s16x4 __attribute__((ext_vector_type(4)));
typedef __bf16 bf16x2 __attribute__((ext_vector_type(2)));
typedef unsigned u32x4 __attribute__((ext_vector_type(4)));
typedef unsigned u32x2 __attribute__((ext_vector_type(2)));
typedef __attribute__((address_space(3))) s16x4* lds_s16x4_p;
typedef __attribute__((address_space(3))) s16x8* lds_s16x8_p;
typedef __attribute__((address_space(3))) f32x4* lds_f32x4_p;
typedef __attribute__((address_space(3))) f32x2* lds_f32x2_p;

#define DI __device__ __forceinline__
DI unsigned cvtpk(float lo, float hi) { f32x2 v = {lo, hi}; bf16x2 b = __builtin_convertvector(v, bf16x2); return __builtin_bit_cast(unsigned, b); }
DI float swap_max(float x) { auto r = __builtin_amdgcn_permlane32_swap(__float_as_uint(x), __float_as_uint(x), false, false); return fmaxf(__uint_as_float(r[0]), __uint_as_float(r[1])); }
DI float swap_add(float x) { auto r = __builtin_amdgcn_permlane32_swap(__float_as_uint(x), __float_as_uint(x), false, false); return __uint_as_float(r[0]) + __uint_as_float(r[1]); }

DI void dma4(unsigned m0v, unsigned v0, unsigned v1, unsigned v2, unsigned v3, const void* sbase) {
  asm volatile("s_mov_b32 m0, %0\n\ts_nop 0\n\t"
               "global_load_lds_dwordx4 %1, %5\n\t"
               "global_load_lds_dwordx4 %2, %5 offset:1024\n\t"
               "global_load_lds_dwordx4 %3, %5 offset:2048\n\t"
               "global_load_lds_dwordx4 %4, %5 offset:3072"
               :: "s"(m0v), "v"(v0), "v"(v1), "v"(v2), "v"(v3), "s"(sbase) : "memory");
}
#define WAITVM(n) asm volatile("s_waitcnt vmcnt(" #n ")" ::: "memory")
#define BARRIER() asm volatile("s_waitcnt lgkmcnt(0)\n\ts_barrier" ::: "memory")

__global__ __launch_bounds__(NTHR, 3)
void gqa_fwd(const __hip_bfloat16* __restrict__ Q, const __hip_bfloat16* __restrict__ K, const __hip_bfloat16* __restrict__ V,
             __hip_bfloat16* __restrict__ O) {
  extern __shared__ __attribute__((aligned(16))) char shm[];
  const unsigned lds0 = (unsigned)(uintptr_t)(__attribute__((address_space(3))) char*)shm;
  const int tid = threadIdx.x, lane = tid & 63;
  const int wid = __builtin_amdgcn_readfirstlane(tid >> 6);
  const int rg = wid >> 2, kg = wid & 3;
  const int r32 = lane & 31, hi = lane >> 5;
  const int bid = blockIdx.x, xcd = bid & 7, slotid = bid >> 3;
  const int pair = xcd * 16 + (slotid >> 1), member = slotid & 1;
  const int bq = pair * 2 + member;
  const char* Kh = (const char*)K + (size_t)pair * SK * D * 2;
  const char* Vh = (const char*)V + (size_t)pair * SK * D * 2;

  s16x8 qf[8];
  {
    const char* qp = (const char*)Q + ((size_t)bq * SQ + rg * 32 + r32) * (D * 2) + hi * 16;
    asm volatile("global_load_dwordx4 %0, %8, off\n\t"
                 "global_load_dwordx4 %1, %8, off offset:32\n\t"
                 "global_load_dwordx4 %2, %8, off offset:64\n\t"
                 "global_load_dwordx4 %3, %8, off offset:96\n\t"
                 "global_load_dwordx4 %4, %8, off offset:128\n\t"
                 "global_load_dwordx4 %5, %8, off offset:160\n\t"
                 "global_load_dwordx4 %6, %8, off offset:192\n\t"
                 "global_load_dwordx4 %7, %8, off offset:224"
                 : "=&v"(qf[0]), "=&v"(qf[1]), "=&v"(qf[2]), "=&v"(qf[3]), "=&v"(qf[4]), "=&v"(qf[5]), "=&v"(qf[6]), "=&v"(qf[7])
                 : "v"(qp) : "memory");
  }
  const bool ldK = (rg != 1), ldV = (rg != 0);
  const int pc0 = kg * 8 + (rg == 2 ? 4 : 0);
  const unsigned lrow = lane >> 4, lch = lane & 15;
  const unsigned ksrc = (unsigned)pc0 * 1024u + lrow * 256u + 16u * (lch ^ lrow);
  const unsigned vsrc = (unsigned)pc0 * 1024u + lrow * 256u + 16u * (lch ^ (lrow << 2));
  const unsigned m0K = lds0 + LDS_K + pc0 * 1024u, m0V = lds0 + LDS_V + pc0 * 1024u;
  auto issueK = [&](int t) { dma4(m0K + (t & 1) * SLOT, ksrc, ksrc ^ 64u, ksrc ^ 128u, ksrc ^ 192u, Kh + (size_t)t * SLOT); };
  auto issueV = [&](int t) { dma4(m0V + (t & 1) * SLOT, vsrc, vsrc, vsrc, vsrc, Vh + (size_t)t * SLOT); };
  if (ldK) issueK(0);
  if (ldV) issueV(0);
  if (ldK) issueK(1);
  if (ldV) issueV(1);
  asm volatile("s_waitcnt vmcnt(8)"  : "+v"(qf[0]), "+v"(qf[1]), "+v"(qf[2]), "+v"(qf[3]), "+v"(qf[4]), "+v"(qf[5]), "+v"(qf[6]), "+v"(qf[7]) :: "memory");

  const unsigned kaddr = lds0 + LDS_K + (kg * 32 + r32) * 256u + 16u * ((unsigned)hi ^ (r32 & 15u));
  const unsigned tq = (lane >> 2) & 3, tg = (lane >> 4) & 1, tp = lane & 3;
  const unsigned vaddr = lds0 + LDS_V + kg * 8192u + (4u * hi + tq) * 256u + 64u * tq + 32u * tg + 8u * tp;

  f32x16 o[4];
#pragma unroll
  for (int i = 0; i < 4; ++i)
#pragma unroll
    for (int r = 0; r < 16; ++r) o[i][r] = 0.f;
  float m = -__builtin_inff(), l = 0.f;

#pragma nounroll
  for (int t = 0; t < NT; ++t) {
    const unsigned so = (t & 1) * SLOT;
    if (rg == 0) { if (t + 1 < NT) WAITVM(4); else WAITVM(0); }
    else if (rg == 2) { if (t + 1 < NT) WAITVM(8); else WAITVM(4); }
    BARRIER();
    if (ldV && t >= 1 && t + 1 < NT) issueV(t + 1);
    f32x16 s;
#pragma unroll
    for (int r = 0; r < 16; ++r) s[r] = 0.f;
#pragma unroll
    for (int d0 = 0; d0 < 8; ++d0) {
      const s16x8 kf = *(lds_s16x8_p)(uintptr_t)((kaddr + so) ^ (32u * d0));
      s = __builtin_amdgcn_mfma_f32_32x32x16_bf16(kf, qf[d0], s, 0, 0, 0);
    }
    float tmax = fmaxf(s[0], s[1]);
#pragma unroll
    for (int r = 2; r < 16; ++r) tmax = fmaxf(tmax, s[r]);
    tmax = swap_max(tmax) * CSC;
    if (__builtin_expect(__any(tmax - m > THR), 0)) {
      const float mn = fmaxf(m, tmax);
      const float alpha = __builtin_amdgcn_exp2f(m - mn);
      m = mn; l *= alpha;
#pragma unroll
      for (int i = 0; i < 4; ++i)
#pragma unroll
        for (int r = 0; r < 16; ++r) o[i][r] *= alpha;
    }
    const float nm = -m;
#pragma unroll
    for (int r = 0; r < 16; ++r) s[r] = __builtin_amdgcn_exp2f(fmaf(s[r], CSC, nm));
    float ps = 0.f;
#pragma unroll
    for (int r = 0; r < 16; ++r) ps += s[r];
    l += ps;
    s16x8 pk[2];
#pragma unroll
    for (int ks = 0; ks < 2; ++ks) {
      u32x4 w = {cvtpk(s[8 * ks], s[8 * ks + 1]), cvtpk(s[8 * ks + 2], s[8 * ks + 3]), cvtpk(s[8 * ks + 4], s[8 * ks + 5]), cvtpk(s[8 * ks + 6], s[8 * ks + 7])};
      pk[ks] = __builtin_bit_cast(s16x8, w);
    }
    if (rg == 1) { if (t + 1 < NT) WAITVM(4); else WAITVM(0); }
    else if (rg == 2) { if (t + 1 < NT) WAITVM(8); else WAITVM(0); }
    BARRIER();
    if (ldK && t + 2 < NT) issueK(t + 2);
#pragma unroll
    for (int db = 0; db < 4; ++db) {
      const unsigned va = (vaddr + so) ^ (64u * db);
#pragma unroll
      for (int ks = 0; ks < 2; ++ks) {
        const s16x4 lo = __builtin_amdgcn_ds_read_tr16_b64_v4i16((lds_s16x4_p)(uintptr_t)(va + ks * 4096u));
        const s16x4 up = __builtin_amdgcn_ds_read_tr16_b64_v4i16((lds_s16x4_p)(uintptr_t)(va + ks * 4096u + 2048u));
        const s16x8 vf = __builtin_shufflevector(lo, up, 0, 1, 2, 3, 4, 5, 6, 7);
        o[db] = __builtin_amdgcn_mfma_f32_32x32x16_bf16(vf, pk[ks], o[db], 0, 0, 0);
      }
    }
  }

  l = swap_add(l);
  if (hi == 0) { f32x2 ml = {m, l}; *(lds_f32x2_p)(uintptr_t)(lds0 + LDS_ML + (unsigned)((rg * 4 + kg) * 32 + r32) * 8u) = ml; }
  BARRIER();
  float f;
  {
    f32x2 ml[4];
#pragma unroll
    for (int k = 0; k < 4; ++k) ml[k] = *(lds_f32x2_p)(uintptr_t)(lds0 + LDS_ML + (unsigned)((rg * 4 + k) * 32 + r32) * 8u);
    const float M = fmaxf(fmaxf(ml[0][0], ml[1][0]), fmaxf(ml[2][0], ml[3][0]));
    float L = 0.f;
#pragma unroll
    for (int k = 0; k < 4; ++k) L += ml[k][1] * __builtin_amdgcn_exp2f(ml[k][0] - M);
    f = __builtin_amdgcn_exp2f(m - M) / L;
  }
#pragma unroll
  for (int db = 0; db < 4; ++db) {
    if (db != kg) {
      const unsigned src = (unsigned)(kg - db - 1) & 3u;
      const unsigned base = lds0 + ((unsigned)(rg * 4 + db) * 3u + src) * 4096u + lane * 16u;
#pragma unroll
      for (int g = 0; g < 4; ++g) {
        f32x4 w = {o[db][4 * g] * f, o[db][4 * g + 1] * f, o[db][4 * g + 2] * f, o[db][4 * g + 3] * f};
        *(lds_f32x4_p)(uintptr_t)(base + g * 1024u) = w;
      }
    }
  }
  BARRIER();
  f32x16 acc;
#pragma unroll
  for (int r = 0; r < 16; ++r) acc[r] = (kg == 0 ? o[0][r] : kg == 1 ? o[1][r] : kg == 2 ? o[2][r] : o[3][r]) * f;
#pragma unroll
  for (int src = 0; src < 3; ++src) {
    const unsigned base = lds0 + ((unsigned)(rg * 4 + kg) * 3u + src) * 4096u + lane * 16u;
#pragma unroll
    for (int g = 0; g < 4; ++g) {
      const f32x4 w = *(lds_f32x4_p)(uintptr_t)(base + g * 1024u);
      acc[4 * g] += w[0]; acc[4 * g + 1] += w[1]; acc[4 * g + 2] += w[2]; acc[4 * g + 3] += w[3];
    }
  }
  int lane2 = lane; asm volatile("" : "+v"(lane2));
  char* op = (char*)O + ((size_t)bq * SQ + rg * 32 + (lane2 & 31)) * (D * 2) + kg * 64 + (lane2 >> 5) * 8;
#pragma unroll
  for (int g = 0; g < 4; ++g) {
    u32x2 w = {cvtpk(acc[4 * g], acc[4 * g + 1]), cvtpk(acc[4 * g + 2], acc[4 * g + 3])};
    *(u32x2*)(op + g * 16) = w;
  }
}
}

extern "C" void kernel_launch(void* const* d_in, const int* in_sizes, int n_in,
                              void* d_out, int out_size, void* d_ws, size_t ws_size,
                              hipStream_t stream) {
  (void)in_sizes; (void)n_in; (void)out_size; (void)d_ws; (void)ws_size;
  (void)hipFuncSetAttribute((const void*)gqa_fwd, hipFuncAttributeMaxDynamicSharedMemorySize, LDS_BYTES);
  hipLaunchKernelGGL(gqa_fwd, dim3(NB * HQ), dim3(NTHR), LDS_BYTES, stream,
                     (const __hip_bfloat16*)d_in[0], (const __hip_bfloat16*)d_in[1], (const __hip_bfloat16*)d_in[2], (__hip_bfloat16*)d_out);
}
```

```cpp
#include <hip/hip_runtime.h>
#include <hip/hip_bf16.h>
#include <stdint.h>
#include <type_traits>

namespace {
constexpr int NB = 8, HQ = 32, HKV = 16, SQ = 96, SK = 2048, D = 128;
constexpr int NWAVES = 12, NTHR = NWAVES * 64;
constexpr int TK = 128, NT = SK / TK;
constexpr int SLOT = TK * D * 2;
constexpr int LDS_K = 0, LDS_V = 2 * SLOT;
constexpr int LDS_ML = 0;
constexpr int LDS_OX = 16384;
constexpr int LDS_BYTES = 5 * SLOT;
constexpr float CSC = 0.088388347648318447f * 1.4426950408889634f;
constexpr float THR = 8.0f;

typedef float f32x16 __attribute__((ext_vector_type(16)));
typedef float f32x4 __attribute__((ext_vector_type(4)));
typedef float f32x2 __attribute__((ext_vector_type(2)));
typedef short s16x8 __attribute__((ext_vector_type(8)));
typedef short s16x4 __attribute__((ext_vector_type(4)));
typedef __bf16 bf16x2 __attribute__((ext_vector_type(2)));
typedef unsigned u32x4 __attribute__((ext_vector_type(4)));
typedef unsigned u32x2 __attribute__((ext_vector_type(2)));
typedef __attribute__((address_space(3))) s16x4* lds_s16x4_p;
typedef __attribute__((address_space(3))) s16x8* lds_s16x8_p;
typedef __attribute__((address_space(3))) f32x4* lds_f32x4_p;
typedef __attribute__((address_space(3))) f32x2* lds_f32x2_p;

#define DI __device__ __forceinline__
DI unsigned cvtpk(float lo, float hi) { f32x2 v = {lo, hi}; bf16x2 b = __builtin_convertvector(v, bf16x2); return __builtin_bit_cast(unsigned, b); }
DI float swap_max(float x) { auto r = __builtin_amdgcn_permlane32_swap(__float_as_uint(x), __float_as_uint(x), false, false); return fmaxf(__uint_as_float(r[0]), __uint_as_float(r[1])); }
DI float swap_add(float x) { auto r = __builtin_amdgcn_permlane32_swap(__float_as_uint(x), __float_as_uint(x), false, false); return __uint_as_float(r[0]) + __uint_as_float(r[1]); }

DI void dma1(unsigned m0v, unsigned voff, const void* sbase) {
  asm volatile("s_mov_b32 m0, %0\n\ts_nop 0\n\tglobal_load_lds_dwordx4 %1, %2 nt" :: "s"(m0v), "v"(voff), "s"(sbase) : "memory");
}
DI void dma1_skip2(unsigned m0v, unsigned voff, const void* sbase, int sel) {
  asm volatile("s_cmp_eq_u32 %3, 2\n\ts_cbranch_scc1 1f\n\ts_mov_b32 m0, %0\n\ts_nop 0\n\tglobal_load_lds_dwordx4 %1, %2 nt\n1:"
               :: "s"(m0v), "v"(voff), "s"(sbase), "s"(sel) : "memory", "scc");
}
#define WAITVM(n) asm volatile("s_waitcnt vmcnt(" #n ")" ::: "memory")
#define WAITVM2(a, b, sel) asm volatile("s_cmp_eq_u32 %0, 2\n\ts_cbranch_scc1 1f\n\ts_waitcnt vmcnt(" #a ")\n\ts_branch 2f\n1:\ts_waitcnt vmcnt(" #b ")\n2:" :: "s"(sel) : "memory", "scc")
#define PIN(x) asm volatile("" : "+v"(x))
#define BARRIER() asm volatile("s_waitcnt lgkmcnt(0)\n\ts_barrier" ::: "memory")

__global__ __launch_bounds__(NTHR, 3)
void gqa_fwd(const __hip_bfloat16* __restrict__ Q, const __hip_bfloat16* __restrict__ K, const __hip_bfloat16* __restrict__ V,
             __hip_bfloat16* __restrict__ O) {
  extern __shared__ __attribute__((aligned(16))) char shm[];
  const unsigned lds0 = (unsigned)(uintptr_t)(__attribute__((address_space(3))) char*)shm;
  const int tid = threadIdx.x, lane = tid & 63;
  const int wid = __builtin_amdgcn_readfirstlane(tid >> 6);
  const int rg = wid >> 2, kg = wid & 3;
  const int r32 = lane & 31, hi = lane >> 5;
  const int bid = blockIdx.x, xcd = bid & 7, slotid = bid >> 3;
  const int pair = xcd * 16 + (slotid >> 1), member = slotid & 1;
  const int bq = pair * 2 + member;
  const char* Kh = (const char*)K + (size_t)pair * SK * D * 2;
  const char* Vh = (const char*)V + (size_t)pair * SK * D * 2;

  const unsigned lrow = lane >> 4, lch = lane & 15;
  const unsigned ksrc = lrow * 256u + 16u * (lch ^ lrow);
  const unsigned vsrc = lrow * 256u + 16u * (lch ^ (lrow << 2));
  const unsigned pofs = kg * 8192u + rg * 1024u;
  auto pieceK = [&](int t, int k) {
      const unsigned d = lds0 + LDS_K + (t & 1) * SLOT + pofs + 3072u * k, v = ksrc ^ (64u * ((rg + 3 * k) & 3)); const char* g = Kh + (size_t)t * SLOT + pofs + 3072u * k;
      if (k < 2) dma1(d, v, g); else dma1_skip2(d, v, g, rg); };
  auto pieceV = [&](int t, unsigned vslot, int k) {
      const unsigned d = lds0 + LDS_V + vslot + pofs + 3072u * k; const char* g = Vh + (size_t)t * SLOT + pofs + 3072u * k;
      if (k < 2) dma1(d, vsrc, g); else dma1_skip2(d, vsrc, g, rg); };
  {
    const char* qg = (const char*)Q + ((size_t)bq * SQ + rg * 32) * (D * 2) + kg * 2048;
    const unsigned qd = lds0 + LDS_V + 2 * SLOT + rg * 8192u + kg * 2048u;
    dma1(qd, ksrc ^ (64u * ((2 * kg) & 3)), qg);
    dma1(qd + 1024u, ksrc ^ (64u * ((2 * kg + 1) & 3)), qg + 1024);
  }
#pragma unroll
  for (int k = 0; k < 3; ++k) pieceK(0, k);
#pragma unroll
  for (int k = 0; k < 3; ++k) pieceK(1, k);
#pragma unroll
  for (int k = 0; k < 3; ++k) pieceV(0, 0, k);
  s16x8 qf[8];

  const unsigned kaddr = lds0 + LDS_K + (kg * 32 + r32) * 256u + 16u * ((unsigned)hi ^ (r32 & 15u));
  const unsigned tq = (lane >> 2) & 3, tg = (lane >> 4) & 1, tp = lane & 3;
  const unsigned vaddr = lds0 + LDS_V + kg * 8192u + (4u * hi + tq) * 256u + 64u * tq + 32u * tg + 8u * tp;

  f32x16 o[4];
#pragma unroll
  for (int i = 0; i < 4; ++i)
#pragma unroll
    for (int r = 0; r < 16; ++r) o[i][r] = 0.f;
  float m = -__builtin_inff(), l = 0.f;

#define SB() __builtin_amdgcn_sched_barrier(0)
  s16x8 kf[8];
  auto kread = [&](int i, unsigned ka) { kf[i] = *(lds_s16x8_p)(uintptr_t)(ka ^ (32u * i)); };
  auto body = [&](auto QKc, auto PVc, auto DVc, auto DKc, auto NXTc, const int t, const unsigned vsp, const unsigned vsn, f32x16& sp, f32x16& s) {
    constexpr bool QK = decltype(QKc)::value, PVV = decltype(PVc)::value, DV = decltype(DVc)::value, DK = decltype(DKc)::value, NXT = decltype(NXTc)::value;
    unsigned ka = kaddr + (t & 1) * SLOT, va0 = vaddr + vsp;
    asm volatile("" : "+v"(ka), "+v"(va0));
    u32x4 pw[2];
    const float nm = -m;
    float ps0 = 0.f, ps1 = 0.f;
    if constexpr (QK) {
#pragma unroll
      for (int r = 0; r < 16; ++r) s[r] = 0.f;
    }
    __builtin_amdgcn_s_setprio(0);
    SB();
#pragma unroll
    for (int g = 0; g < 8; ++g) {
      if constexpr (QK) {
        s = __builtin_amdgcn_mfma_f32_32x32x16_bf16(kf[g], qf[g], s, 0, 0, 0);
        if (g + 3 < 8) kread(g + 3, ka);
      }
      if constexpr (PVV) {
        float e0 = __builtin_amdgcn_exp2f(fmaf(sp[2 * g], CSC, nm)), e1 = __builtin_amdgcn_exp2f(fmaf(sp[2 * g + 1], CSC, nm));
        PIN(e0); PIN(e1);
        sp[2 * g] = e0; sp[2 * g + 1] = e1;
        if (g > 0) { unsigned w = cvtpk(sp[2 * g - 2], sp[2 * g - 1]); ps0 += sp[2 * g - 2]; ps1 += sp[2 * g - 1]; PIN(w); PIN(ps0); PIN(ps1); pw[(g - 1) >> 2][(g - 1) & 3] = w; }
      }
      SB();
      if constexpr (DV) { if (g == 1 || g == 4 || g == 7) { pieceV(t, vsn, g / 3); SB(); } }
    }
    if constexpr (PVV) { unsigned w = cvtpk(sp[14], sp[15]); PIN(w); pw[1][3] = w; l += (ps0 + sp[14]) + (ps1 + sp[15]); PIN(l); }
    if constexpr (QK) {
      if constexpr (NXT) WAITVM2(3, 2, rg); else WAITVM(0);
      BARRIER();
    }
    __builtin_amdgcn_s_setprio(1);
    s16x8 vf[8];
    auto vread = [&](int i) {
      const unsigned va = (va0 ^ (64u * (i & 3))) + (i >> 2) * 4096u;
      const s16x4 lo = __builtin_amdgcn_ds_read_tr16_b64_v4i16((lds_s16x4_p)(uintptr_t)(va));
      const s16x4 up = __builtin_amdgcn_ds_read_tr16_b64_v4i16((lds_s16x4_p)(uintptr_t)(va + 2048u));
      vf[i] = __builtin_shufflevector(lo, up, 0, 1, 2, 3, 4, 5, 6, 7);
    };
    if constexpr (PVV) { vread(0); vread(1); vread(2); }
    SB();
    float mx0 = 0.f, mx1 = 0.f;
    if constexpr (QK) {
      mx0 = fmaxf(fmaxf(s[0], s[1]), s[2]); mx1 = fmaxf(fmaxf(s[3], s[4]), s[5]);
      mx0 = fmaxf(fmaxf(mx0, s[6]), s[7]); mx1 = fmaxf(fmaxf(mx1, s[8]), s[9]);
      mx0 = fmaxf(fmaxf(mx0, s[10]), s[11]); mx1 = fmaxf(fmaxf(mx1, s[12]), s[13]);
      mx0 = fmaxf(fmaxf(mx0, s[14]), s[15]); mx0 = fmaxf(mx0, mx1);
      mx0 = swap_max(mx0) * CSC; PIN(mx0);
    }
    SB();
    unsigned kn = kaddr + ((t + 1) & 1) * SLOT;
    asm volatile("" : "+v"(kn));
#pragma unroll
    for (int g = 0; g < 8; ++g) {
      if constexpr (PVV) {
        o[g & 3] = __builtin_amdgcn_mfma_f32_32x32x16_bf16(vf[g], __builtin_bit_cast(s16x8, pw[g >> 2]), o[g & 3], 0, 0, 0);
        if (g + 3 < 8) vread(g + 3);
      }
      if constexpr (QK && NXT) { if (g >= 5) kread(g - 5, kn); }
      SB();
      if constexpr (DK) { if (g == 0 || g == 3 || g == 6) { pieceK(t + 2, g / 3); SB(); } }
      if constexpr (QK && NXT && !DK) { if (g == 0 || g == 3 || g == 6) { pieceV(t + 1, ((NT - 1) % 3) * SLOT, g / 3); SB(); } }
    }
    if constexpr (QK) {
      if (__builtin_expect(__any(mx0 - m > THR), 0)) {
        const float mn = fmaxf(m, mx0);
        const float alpha = __builtin_amdgcn_exp2f(m - mn);
        m = mn; l *= alpha;
#pragma unroll
        for (int i = 0; i < 4; ++i)
#pragma unroll
          for (int r = 0; r < 16; ++r) o[i][r] *= alpha;
      }
    }
  };
  {
    using T = std::true_type; using F = std::false_type;
    f32x16 sA, sB;
    WAITVM2(6, 4, rg);
    BARRIER();
    {
      const unsigned qa = lds0 + LDS_V + 2 * SLOT + rg * 8192u + r32 * 256u + 16u * ((unsigned)hi ^ (r32 & 15u));
#pragma unroll
      for (int d0 = 0; d0 < 8; ++d0) qf[d0] = *(lds_s16x8_p)(uintptr_t)(qa ^ (32u * d0));
      kread(0, kaddr); kread(1, kaddr); kread(2, kaddr);
    }
    body(T{}, F{}, F{}, T{}, T{}, 0, 0, 0, sB, sA);
    unsigned vsp = 0, vsn = SLOT;
#pragma nounroll
    for (int t = 1; t + 1 < NT - 2; t += 2) {
      body(T{}, T{}, T{}, T{}, T{}, t, vsp, vsn, sA, sB);
      vsp = vsn; vsn = vsn == 2 * SLOT ? 0 : vsn + SLOT;
      body(T{}, T{}, T{}, T{}, T{}, t + 1, vsp, vsn, sB, sA);
      vsp = vsn; vsn = vsn == 2 * SLOT ? 0 : vsn + SLOT;
    }
    body(T{}, T{}, T{}, T{}, T{}, NT - 3, ((NT - 4) % 3) * SLOT, ((NT - 3) % 3) * SLOT, sA, sB);
    body(T{}, T{}, T{}, F{}, T{}, NT - 2, ((NT - 3) % 3) * SLOT, ((NT - 2) % 3) * SLOT, sB, sA);
    body(T{}, T{}, F{}, F{}, F{}, NT - 1, ((NT - 2) % 3) * SLOT, 0, sA, sB);
    body(F{}, T{}, F{}, F{}, F{}, NT, ((NT - 1) % 3) * SLOT, 0, sB, sA);
  }

  l = swap_add(l);
  if (hi == 0) { f32x2 ml = {m, l}; *(lds_f32x2_p)(uintptr_t)(lds0 + LDS_ML + (unsigned)((rg * 4 + kg) * 32 + r32) * 8u) = ml; }
  BARRIER();
  float f;
  {
    f32x2 ml[4];
#pragma unroll
    for (int k = 0; k < 4; ++k) ml[k] = *(lds_f32x2_p)(uintptr_t)(lds0 + LDS_ML + (unsigned)((rg * 4 + k) * 32 + r32) * 8u);
    const float M = fmaxf(fmaxf(ml[0][0], ml[1][0]), fmaxf(ml[2][0], ml[3][0]));
    float L = 0.f;
#pragma unroll
    for (int k = 0; k < 4; ++k) L += ml[k][1] * __builtin_amdgcn_exp2f(ml[k][0] - M);
    f = __builtin_amdgcn_exp2f(m - M) / L;
  }
  int lane2 = lane; asm volatile("" : "+v"(lane2));
  auto fin = [&](auto KGc) {
    constexpr int KG = decltype(KGc)::value;
#pragma unroll
    for (int db = 0; db < 4; ++db) {
      if (db != KG) {
        constexpr unsigned dummy = 0; (void)dummy;
        const unsigned src = (unsigned)(KG - db - 1) & 3u;
        const unsigned base = lds0 + LDS_OX + ((unsigned)(rg * 4 + db) * 3u + src) * 4096u + lane2 * 16u;
#pragma unroll
        for (int g = 0; g < 4; ++g) {
          f32x4 w = {o[db][4 * g] * f, o[db][4 * g + 1] * f, o[db][4 * g + 2] * f, o[db][4 * g + 3] * f};
          *(lds_f32x4_p)(uintptr_t)(base + g * 1024u) = w;
        }
      }
    }
    BARRIER();
    f32x16 acc;
#pragma unroll
    for (int r = 0; r < 16; ++r) acc[r] = o[KG][r] * f;
#pragma unroll
    for (int src = 0; src < 3; ++src) {
      const unsigned base = lds0 + LDS_OX + ((unsigned)(rg * 4 + KG) * 3u + src) * 4096u + lane2 * 16u;
#pragma unroll
      for (int g = 0; g < 4; ++g) {
        const f32x4 w = *(lds_f32x4_p)(uintptr_t)(base + g * 1024u);
        acc[4 * g] += w[0]; acc[4 * g + 1] += w[1]; acc[4 * g + 2] += w[2]; acc[4 * g + 3] += w[3];
      }
    }
    char* op = (char*)O + ((size_t)bq * SQ + rg * 32 + (lane2 & 31)) * (D * 2) + KG * 64 + (lane2 >> 5) * 8;
#pragma unroll
    for (int g = 0; g < 4; ++g) {
      u32x2 w = {cvtpk(acc[4 * g], acc[4 * g + 1]), cvtpk(acc[4 * g + 2], acc[4 * g + 3])};
      *(u32x2*)(op + g * 16) = w;
    }
  };
  if (kg == 0) fin(std::integral_constant<int, 0>{});
  else if (kg == 1) fin(std::integral_constant<int, 1>{});
  else if (kg == 2) fin(std::integral_constant<int, 2>{});
  else fin(std::integral_constant<int, 3>{});
}
}

extern "C" void kernel_launch(void* const* d_in, const int* in_sizes, int n_in,
                              void* d_out, int out_size, void* d_ws, size_t ws_size,
                              hipStream_t stream) {
  (void)in_sizes; (void)n_in; (void)out_size; (void)d_ws; (void)ws_size;
  (void)hipFuncSetAttribute((const void*)gqa_fwd, hipFuncAttributeMaxDynamicSharedMemorySize, LDS_BYTES);
  hipLaunchKernelGGL(gqa_fwd, dim3(NB * HQ), dim3(NTHR), LDS_BYTES, stream,
                     (const __hip_bfloat16*)d_in[0], (const __hip_bfloat16*)d_in[1], (const __hip_bfloat16*)d_in[2], (__hip_bfloat16*)d_out);
}
```

```cpp
#include <hip/hip_runtime.h>
#include <hip/hip_bf16.h>
#include <stdint.h>
#include <type_traits>

namespace {
constexpr int NB = 8, HQ = 32, HKV = 16, SQ = 96, SK = 2048, D = 128;
constexpr int NWAVES = 12, NTHR = NWAVES * 64;
constexpr int TK = 128, NT = SK / TK;
constexpr int SLOT = TK * D * 2;
constexpr int LDS_K = 0, LDS_V = 2 * SLOT;
constexpr int LDS_ML = 0;
constexpr int LDS_OX = 16384;
constexpr int LDS_BYTES = 5 * SLOT;
constexpr float CSC = 0.088388347648318447f * 1.4426950408889634f;
constexpr float THR = 8.0f;

typedef float f32x16 __attribute__((ext_vector_type(16)));
typedef float f32x4 __attribute__((ext_vector_type(4)));
typedef float f32x2 __attribute__((ext_vector_type(2)));
typedef short s16x8 __attribute__((ext_vector_type(8)));
typedef short s16x4 __attribute__((ext_vector_type(4)));
typedef __bf16 bf16x2 __attribute__((ext_vector_type(2)));
typedef unsigned u32x4 __attribute__((ext_vector_type(4)));
typedef unsigned u32x2 __attribute__((ext_vector_type(2)));
typedef __attribute__((address_space(3))) s16x4* lds_s16x4_p;
typedef __attribute__((address_space(3))) s16x8* lds_s16x8_p;
typedef __attribute__((address_space(3))) f32x4* lds_f32x4_p;
typedef __attribute__((address_space(3))) f32x2* lds_f32x2_p;

#define DI __device__ __forceinline__
DI unsigned cvtpk(float lo, float hi) { f32x2 v = {lo, hi}; bf16x2 b = __builtin_convertvector(v, bf16x2); return __builtin_bit_cast(unsigned, b); }
DI float swap_max(float x) { auto r = __builtin_amdgcn_permlane32_swap(__float_as_uint(x), __float_as_uint(x), false, false); return fmaxf(__uint_as_float(r[0]), __uint_as_float(r[1])); }
DI float swap_add(float x) { auto r = __builtin_amdgcn_permlane32_swap(__float_as_uint(x), __float_as_uint(x), false, false); return __uint_as_float(r[0]) + __uint_as_float(r[1]); }

DI void dma1(unsigned m0v, unsigned voff, const void* sbase) {
  asm volatile("s_mov_b32 m0, %0\n\ts_nop 0\n\tglobal_load_lds_dwordx4 %1, %2 nt" :: "s"(m0v), "v"(voff), "s"(sbase) : "memory");
}
DI void dma1_skip2(unsigned m0v, unsigned voff, const void* sbase, int sel) {
  asm volatile("s_cmp_eq_u32 %3, 2\n\ts_cbranch_scc1 1f\n\ts_mov_b32 m0, %0\n\ts_nop 0\n\tglobal_load_lds_dwordx4 %1, %2 nt\n1:"
               :: "s"(m0v), "v"(voff), "s"(sbase), "s"(sel) : "memory", "scc");
}
#define WAITVM(n) asm volatile("s_waitcnt vmcnt(" #n ")" ::: "memory")
#define WAITVM2(a, b, sel) asm volatile("s_cmp_eq_u32 %0, 2\n\ts_cbranch_scc1 1f\n\ts_waitcnt vmcnt(" #a ")\n\ts_branch 2f\n1:\ts_waitcnt vmcnt(" #b ")\n2:" :: "s"(sel) : "memory", "scc")
#define PIN(x) asm volatile("" : "+v"(x))
#define BARRIER() asm volatile("s_waitcnt lgkmcnt(0)\n\ts_barrier" ::: "memory")

__global__ __launch_bounds__(NTHR, 3)
void gqa_fwd(const __hip_bfloat16* __restrict__ Q, const __hip_bfloat16* __restrict__ K, const __hip_bfloat16* __restrict__ V,
             __hip_bfloat16* __restrict__ O) {
  extern __shared__ __attribute__((aligned(16))) char shm[];
  const unsigned lds0 = (unsigned)(uintptr_t)(__attribute__((address_space(3))) char*)shm;
  const int tid = threadIdx.x, lane = tid & 63;
  const int wid = __builtin_amdgcn_readfirstlane(tid >> 6);
  const int rg = wid >> 2, kg = wid & 3;
  const int r32 = lane & 31, hi = lane >> 5;
  const int bid = blockIdx.x, xcd = bid & 7, slotid = bid >> 3;
  const int pair = xcd * 16 + (slotid >> 1), member = slotid & 1;
  const int bq = pair * 2 + member;
  const char* Kh = (const char*)K + (size_t)pair * SK * D * 2;
  const char* Vh = (const char*)V + (size_t)pair * SK * D * 2;

  const unsigned lrow = lane >> 4, lch = lane & 15;
  const unsigned ksrc = lrow * 256u + 16u * (lch ^ lrow);
  const unsigned vsrc = lrow * 256u + 16u * (lch ^ (lrow << 2));
  const unsigned pofs = kg * 8192u + rg * 1024u;
  auto pieceK = [&](int t, int k) {
      const unsigned d = lds0 + LDS_K + (t & 1) * SLOT + pofs + 3072u * k, v = ksrc ^ (64u * ((rg + 3 * k) & 3)); const char* g = Kh + (size_t)t * SLOT + pofs + 3072u * k;
      if (k < 2) dma1(d, v, g); else dma1_skip2(d, v, g, rg); };
  auto pieceV = [&](int t, unsigned vslot, int k) {
      const unsigned d = lds0 + LDS_V + vslot + pofs + 3072u * k; const char* g = Vh + (size_t)t * SLOT + pofs + 3072u * k;
      if (k < 2) dma1(d, vsrc, g); else dma1_skip2(d, vsrc, g, rg); };
  {
    const char* qg = (const char*)Q + ((size_t)bq * SQ + rg * 32) * (D * 2) + kg * 2048;
    const unsigned qd = lds0 + LDS_V + 2 * SLOT + rg * 8192u + kg * 2048u;
    dma1(qd, ksrc ^ (64u * ((2 * kg) & 3)), qg);
    dma1(qd + 1024u, ksrc ^ (64u * ((2 * kg + 1) & 3)), qg + 1024);
  }
#pragma unroll
  for (int k = 0; k < 3; ++k) pieceK(0, k);
#pragma unroll
  for (int k = 0; k < 3; ++k) pieceV(0, 0, k);
#pragma unroll
  for (int k = 0; k < 3; ++k) pieceK(1, k);
  s16x8 qf[8];

  const unsigned kaddr = lds0 + LDS_K + (kg * 32 + r32) * 256u + 16u * ((unsigned)hi ^ (r32 & 15u));
  const unsigned tq = (lane >> 2) & 3, tg = (lane >> 4) & 1, tp = lane & 3;
  const unsigned vaddr = lds0 + LDS_V + kg * 8192u + (4u * hi + tq) * 256u + 64u * tq + 32u * tg + 8u * tp;

  f32x16 o[4];
#pragma unroll
  for (int i = 0; i < 4; ++i)
#pragma unroll
    for (int r = 0; r < 16; ++r) o[i][r] = 0.f;
  float m = -__builtin_inff(), l = 0.f;

#define SB() __builtin_amdgcn_sched_barrier(0)
  auto body = [&](auto QKc, auto PVc, auto DVc, auto DKc, auto WNc, const int t, const unsigned vsp, const unsigned vsn, f32x16& sp, f32x16& s) {
    constexpr bool QK = decltype(QKc)::value, PVV = decltype(PVc)::value, DV = decltype(DVc)::value, DK = decltype(DKc)::value;
    constexpr int WN = decltype(WNc)::value;
    unsigned ka = kaddr + (t & 1) * SLOT, va0 = vaddr + vsp;
    asm volatile("" : "+v"(ka), "+v"(va0));
    if constexpr (WN == 2) WAITVM2(6, 4, rg);
    if constexpr (WN == 1) WAITVM2(3, 2, rg);
    if constexpr (WN == 0) WAITVM(0);
    BARRIER();
    if constexpr (!PVV) {
      const unsigned qa = lds0 + LDS_V + 2 * SLOT + rg * 8192u + r32 * 256u + 16u * ((unsigned)hi ^ (r32 & 15u));
#pragma unroll
      for (int d0 = 0; d0 < 8; ++d0) qf[d0] = *(lds_s16x8_p)(uintptr_t)(qa ^ (32u * d0));
    }
    s16x8 kf[8];
    u32x4 pw[2];
    const float nm = -m;
    float ps0 = 0.f, ps1 = 0.f;
    if constexpr (QK) {
#pragma unroll
      for (int r = 0; r < 16; ++r) s[r] = 0.f;
#pragma unroll
      for (int d0 = 0; d0 < 3; ++d0) kf[d0] = *(lds_s16x8_p)(uintptr_t)(ka ^ (32u * d0));
    }
    SB();
#pragma unroll
    for (int g = 0; g < 8; ++g) {
      if constexpr (PVV) {
        float e0 = __builtin_amdgcn_exp2f(fmaf(sp[2 * g], CSC, nm)), e1 = __builtin_amdgcn_exp2f(fmaf(sp[2 * g + 1], CSC, nm));
        PIN(e0); PIN(e1);
        sp[2 * g] = e0; sp[2 * g + 1] = e1;
        if (g > 0) { unsigned w = cvtpk(sp[2 * g - 2], sp[2 * g - 1]); ps0 += sp[2 * g - 2]; ps1 += sp[2 * g - 1]; PIN(w); PIN(ps0); PIN(ps1); pw[(g - 1) >> 2][(g - 1) & 3] = w; }
      }
      if constexpr (QK) {
        if (g + 3 < 8) kf[g + 3] = *(lds_s16x8_p)(uintptr_t)(ka ^ (32u * (g + 3)));
        s = __builtin_amdgcn_mfma_f32_32x32x16_bf16(kf[g], qf[g], s, 0, 0, 0);
      }
      SB();
      if constexpr (DV) { if (g == 1 || g == 3 || g == 5) { pieceV(t + 1, vsn, g >> 1); SB(); } }
    }
    if constexpr (PVV) { unsigned w = cvtpk(sp[14], sp[15]); PIN(w); pw[1][3] = w; l += (ps0 + sp[14]) + (ps1 + sp[15]); PIN(l); }
    s16x8 vf[8];
    auto vread = [&](int i) {
      const unsigned va = (va0 ^ (64u * (i & 3))) + (i >> 2) * 4096u;
      const s16x4 lo = __builtin_amdgcn_ds_read_tr16_b64_v4i16((lds_s16x4_p)(uintptr_t)(va));
      const s16x4 up = __builtin_amdgcn_ds_read_tr16_b64_v4i16((lds_s16x4_p)(uintptr_t)(va + 2048u));
      vf[i] = __builtin_shufflevector(lo, up, 0, 1, 2, 3, 4, 5, 6, 7);
    };
    if constexpr (PVV) { vread(0); vread(1); vread(2); }
    if constexpr (QK) BARRIER();
    SB();
    float mx0 = 0.f, mx1 = 0.f;
#pragma unroll
    for (int g = 0; g < 8; ++g) {
      if constexpr (PVV) {
        if (g + 3 < 8) vread(g + 3);
        o[g & 3] = __builtin_amdgcn_mfma_f32_32x32x16_bf16(vf[g], __builtin_bit_cast(s16x8, pw[g >> 2]), o[g & 3], 0, 0, 0);
      }
      if constexpr (QK) {
        if (g == 2) { mx0 = fmaxf(fmaxf(s[0], s[1]), s[2]); mx1 = fmaxf(fmaxf(s[3], s[4]), s[5]); PIN(mx0); PIN(mx1); }
        if (g == 3) { mx0 = fmaxf(fmaxf(mx0, s[6]), s[7]); mx1 = fmaxf(fmaxf(mx1, s[8]), s[9]); PIN(mx0); PIN(mx1); }
        if (g == 4) { mx0 = fmaxf(fmaxf(mx0, s[10]), s[11]); mx1 = fmaxf(fmaxf(mx1, s[12]), s[13]); PIN(mx0); PIN(mx1); }
        if (g == 5) { mx0 = fmaxf(fmaxf(mx0, s[14]), s[15]); mx0 = fmaxf(mx0, mx1); PIN(mx0); }
        if (g == 6) { mx0 = swap_max(mx0) * CSC; PIN(mx0); }
      }
      SB();
      if constexpr (DK) { if (g == 0 || g == 2 || g == 4) { pieceK(t + 2, g >> 1); SB(); } }
    }
    if constexpr (QK) {
      if (__builtin_expect(__any(mx0 - m > THR), 0)) {
        const float mn = fmaxf(m, mx0);
        const float alpha = __builtin_amdgcn_exp2f(m - mn);
        m = mn; l *= alpha;
#pragma unroll
        for (int i = 0; i < 4; ++i)
#pragma unroll
          for (int r = 0; r < 16; ++r) o[i][r] *= alpha;
      }
    }
  };
  {
    using T = std::true_type; using F = std::false_type;
    using I0 = std::integral_constant<int, 0>; using I1 = std::integral_constant<int, 1>; using I2 = std::integral_constant<int, 2>;
    f32x16 sA, sB;
    body(T{}, F{}, T{}, T{}, I2{}, 0, 0, SLOT, sB, sA);
    unsigned vsp = 0, vsn = 2 * SLOT;
#pragma nounroll
    for (int t = 1; t + 1 < NT - 2; t += 2) {
      body(T{}, T{}, T{}, T{}, I2{}, t, vsp, vsn, sA, sB);
      vsn = vsp; vsp = vsp == 2 * SLOT ? 0 : vsp + SLOT;
      body(T{}, T{}, T{}, T{}, I2{}, t + 1, vsp, vsn, sB, sA);
      vsn = vsp; vsp = vsp == 2 * SLOT ? 0 : vsp + SLOT;
    }
    body(T{}, T{}, T{}, T{}, I2{}, NT - 3, ((NT - 4) % 3) * SLOT, ((NT - 2) % 3) * SLOT, sA, sB);
    body(T{}, T{}, T{}, F{}, I2{}, NT - 2, ((NT - 3) % 3) * SLOT, ((NT - 1) % 3) * SLOT, sB, sA);
    body(T{}, T{}, F{}, F{}, I1{}, NT - 1, ((NT - 2) % 3) * SLOT, 0, sA, sB);
    body(F{}, T{}, F{}, F{}, I0{}, NT, ((NT - 1) % 3) * SLOT, 0, sB, sA);
  }

  l = swap_add(l);
  if (hi == 0) { f32x2 ml = {m, l}; *(lds_f32x2_p)(uintptr_t)(lds0 + LDS_ML + (unsigned)((rg * 4 + kg) * 32 + r32) * 8u) = ml; }
  BARRIER();
  float f;
  {
    f32x2 ml[4];
#pragma unroll
    for (int k = 0; k < 4; ++k) ml[k] = *(lds_f32x2_p)(uintptr_t)(lds0 + LDS_ML + (unsigned)((rg * 4 + k) * 32 + r32) * 8u);
    const float M = fmaxf(fmaxf(ml[0][0], ml[1][0]), fmaxf(ml[2][0], ml[3][0]));
    float L = 0.f;
#pragma unroll
    for (int k = 0; k < 4; ++k) L += ml[k][1] * __builtin_amdgcn_exp2f(ml[k][0] - M);
    f = __builtin_amdgcn_exp2f(m - M) / L;
  }
  int lane2 = lane; asm volatile("" : "+v"(lane2));
  auto fin = [&](auto KGc) {
    constexpr int KG = decltype(KGc)::value;
#pragma unroll
    for (int db = 0; db < 4; ++db) {
      if (db != KG) {
        constexpr unsigned dummy = 0; (void)dummy;
        const unsigned src = (unsigned)(KG - db - 1) & 3u;
        const unsigned base = lds0 + LDS_OX + ((unsigned)(rg * 4 + db) * 3u + src) * 4096u + lane2 * 16u;
#pragma unroll
        for (int g = 0; g < 4; ++g) {
          f32x4 w = {o[db][4 * g] * f, o[db][4 * g + 1] * f, o[db][4 * g + 2] * f, o[db][4 * g + 3] * f};
          *(lds_f32x4_p)(uintptr_t)(base + g * 1024u) = w;
        }
      }
    }
    BARRIER();
    f32x16 acc;
#pragma unroll
    for (int r = 0; r < 16; ++r) acc[r] = o[KG][r] * f;
#pragma unroll
    for (int src = 0; src < 3; ++src) {
      const unsigned base = lds0 + LDS_OX + ((unsigned)(rg * 4 + KG) * 3u + src) * 4096u + lane2 * 16u;
#pragma unroll
      for (int g = 0; g < 4; ++g) {
        const f32x4 w = *(lds_f32x4_p)(uintptr_t)(base + g * 1024u);
        acc[4 * g] += w[0]; acc[4 * g + 1] += w[1]; acc[4 * g + 2] += w[2]; acc[4 * g + 3] += w[3];
      }
    }
    char* op = (char*)O + ((size_t)bq * SQ + rg * 32 + (lane2 & 31)) * (D * 2) + KG * 64 + (lane2 >> 5) * 8;
#pragma unroll
    for (int g = 0; g < 4; ++g) {
      u32x2 w = {cvtpk(acc[4 * g], acc[4 * g + 1]), cvtpk(acc[4 * g + 2], acc[4 * g + 3])};
      *(u32x2*)(op + g * 16) = w;
    }
  };
  if (kg == 0) fin(std::integral_constant<int, 0>{});
  else if (kg == 1) fin(std::integral_constant<int, 1>{});
  else if (kg == 2) fin(std::integral_constant<int, 2>{});
  else fin(std::integral_constant<int, 3>{});
}
}

extern "C" void kernel_launch(void* const* d_in, const int* in_sizes, int n_in,
                              void* d_out, int out_size, void* d_ws, size_t ws_size,
                              hipStream_t stream) {
  (void)in_sizes; (void)n_in; (void)out_size; (void)d_ws; (void)ws_size;
  (void)hipFuncSetAttribute((const void*)gqa_fwd, hipFuncAttributeMaxDynamicSharedMemorySize, LDS_BYTES);
  hipLaunchKernelGGL(gqa_fwd, dim3(NB * HQ), dim3(NTHR), LDS_BYTES, stream,
                     (const __hip_bfloat16*)d_in[0], (const __hip_bfloat16*)d_in[1], (const __hip_bfloat16*)d_in[2], (__hip_bfloat16*)d_out);
}
```

```cpp
#include <hip/hip_runtime.h>
#include <hip/hip_bf16.h>
#include <stdint.h>
#include <type_traits>

namespace {
constexpr int NB = 8, HQ = 32, HKV = 16, SQ = 96, SK = 2048, D = 128;
constexpr int NWAVES = 12, NTHR = NWAVES * 64;
constexpr int TK = 128, NT = SK / TK;
constexpr int SLOT = TK * D * 2;
constexpr int LDS_K = 0, LDS_V = 2 * SLOT;
constexpr int LDS_ML = 0;
constexpr int LDS_OX = 16384;
constexpr int LDS_BYTES = 5 * SLOT;
constexpr float CSC = 0.088388347648318447f * 1.4426950408889634f;
constexpr float THR = 8.0f;

typedef float f32x16 __attribute__((ext_vector_type(16)));
typedef float f32x4 __attribute__((ext_vector_type(4)));
typedef float f32x2 __attribute__((ext_vector_type(2)));
typedef short s16x8 __attribute__((ext_vector_type(8)));
typedef short s16x4 __attribute__((ext_vector_type(4)));
typedef __bf16 bf16x2 __attribute__((ext_vector_type(2)));
typedef unsigned u32x4 __attribute__((ext_vector_type(4)));
typedef unsigned u32x2 __attribute__((ext_vector_type(2)));
typedef __attribute__((address_space(3))) s16x4* lds_s16x4_p;
typedef __attribute__((address_space(3))) s16x8* lds_s16x8_p;
typedef __attribute__((address_space(3))) f32x4* lds_f32x4_p;
typedef __attribute__((address_space(3))) f32x2* lds_f32x2_p;

#define DI __device__ __forceinline__
DI unsigned cvtpk(float lo, float hi) { f32x2 v = {lo, hi}; bf16x2 b = __builtin_convertvector(v, bf16x2); return __builtin_bit_cast(unsigned, b); }
DI float swap_max(float x) { auto r = __builtin_amdgcn_permlane32_swap(__float_as_uint(x), __float_as_uint(x), false, false); return fmaxf(__uint_as_float(r[0]), __uint_as_float(r[1])); }
DI float swap_add(float x) { auto r = __builtin_amdgcn_permlane32_swap(__float_as_uint(x), __float_as_uint(x), false, false); return __uint_as_float(r[0]) + __uint_as_float(r[1]); }

DI void dma1(unsigned m0v, unsigned voff, const void* sbase) {
  asm volatile("s_mov_b32 m0, %0\n\ts_nop 0\n\tglobal_load_lds_dwordx4 %1, %2 nt" :: "s"(m0v), "v"(voff), "s"(sbase) : "memory");
}
DI void dma1_skip2(unsigned m0v, unsigned voff, const void* sbase, int sel) {
  asm volatile("s_cmp_eq_u32 %3, 2\n\ts_cbranch_scc1 1f\n\ts_mov_b32 m0, %0\n\ts_nop 0\n\tglobal_load_lds_dwordx4 %1, %2 nt\n1:"
               :: "s"(m0v), "v"(voff), "s"(sbase), "s"(sel) : "memory", "scc");
}
#define WAITVM(n) asm volatile("s_waitcnt vmcnt(" #n ")" ::: "memory")
#define WAITVM2(a, b, sel) asm volatile("s_cmp_eq_u32 %0, 2\n\ts_cbranch_scc1 1f\n\ts_waitcnt vmcnt(" #a ")\n\ts_branch 2f\n1:\ts_waitcnt vmcnt(" #b ")\n2:" :: "s"(sel) : "memory", "scc")
#define PIN(x) asm volatile("" : "+v"(x))
#define BARRIER() asm volatile("s_waitcnt lgkmcnt(0)\n\ts_barrier" ::: "memory")

__global__ __launch_bounds__(NTHR, 3)
void gqa_fwd(const __hip_bfloat16* __restrict__ Q, const __hip_bfloat16* __restrict__ K, const __hip_bfloat16* __restrict__ V,
             __hip_bfloat16* __restrict__ O) {
  extern __shared__ __attribute__((aligned(16))) char shm[];
  const unsigned lds0 = (unsigned)(uintptr_t)(__attribute__((address_space(3))) char*)shm;
  const int tid = threadIdx.x, lane = tid & 63;
  const int wid = __builtin_amdgcn_readfirstlane(tid >> 6);
  const int rg = wid >> 2, kg = wid & 3;
  const int r32 = lane & 31, hi = lane >> 5;
  const int bid = blockIdx.x, xcd = bid & 7, slotid = bid >> 3;
  const int pair = xcd * 16 + (slotid >> 1), member = slotid & 1;
  const int bq = pair * 2 + member;
  const char* Kh = (const char*)K + (size_t)pair * SK * D * 2;
  const char* Vh = (const char*)V + (size_t)pair * SK * D * 2;

  const unsigned lrow = lane >> 4, lch = lane & 15;
  const unsigned ksrc = lrow * 256u + 16u * (lch ^ lrow);
  const unsigned vsrc = lrow * 256u + 16u * (lch ^ (lrow << 2));
  const unsigned pofs = kg * 8192u + rg * 1024u;
  auto pieceK = [&](int t, int k) {
      const unsigned d = lds0 + LDS_K + (t & 1) * SLOT + pofs + 3072u * k, v = ksrc ^ (64u * ((rg + 3 * k) & 3)); const char* g = Kh + (size_t)t * SLOT + pofs + 3072u * k;
      if (k < 2) dma1(d, v, g); else dma1_skip2(d, v, g, rg); };
  auto pieceV = [&](int t, unsigned vslot, int k) {
      const unsigned d = lds0 + LDS_V + vslot + pofs + 3072u * k; const char* g = Vh + (size_t)t * SLOT + pofs + 3072u * k;
      if (k < 2) dma1(d, vsrc, g); else dma1_skip2(d, vsrc, g, rg); };
  {
    const char* qg = (const char*)Q + ((size_t)bq * SQ + rg * 32) * (D * 2) + kg * 2048;
    const unsigned qd = lds0 + LDS_V + 2 * SLOT + rg * 8192u + kg * 2048u;
    dma1(qd, ksrc ^ (64u * ((2 * kg) & 3)), qg);
    dma1(qd + 1024u, ksrc ^ (64u * ((2 * kg + 1) & 3)), qg + 1024);
  }
#pragma unroll
  for (int k = 0; k < 3; ++k) pieceK(0, k);
#pragma unroll
  for (int k = 0; k < 3; ++k) pieceK(1, k);
#pragma unroll
  for (int k = 0; k < 3; ++k) pieceV(0, 0, k);
  s16x8 qf[8];

  if (rg == 1) __builtin_amdgcn_s_setprio(1); else if (rg == 2) __builtin_amdgcn_s_setprio(2);
  const unsigned kaddr = lds0 + LDS_K + (kg * 32 + r32) * 256u + 16u * ((unsigned)hi ^ (r32 & 15u));
  const unsigned tq = (lane >> 2) & 3, tg = (lane >> 4) & 1, tp = lane & 3;
  const unsigned vaddr = lds0 + LDS_V + kg * 8192u + (4u * hi + tq) * 256u + 64u * tq + 32u * tg + 8u * tp;

  f32x16 o[4];
#pragma unroll
  for (int i = 0; i < 4; ++i)
#pragma unroll
    for (int r = 0; r < 16; ++r) o[i][r] = 0.f;
  float m = -__builtin_inff(), l = 0.f;

#define SB() __builtin_amdgcn_sched_barrier(0)
  s16x8 kf[8];
  auto kread = [&](int i, unsigned ka) { kf[i] = *(lds_s16x8_p)(uintptr_t)(ka ^ (32u * i)); };
  auto body = [&](auto QKc, auto PVc, auto DVc, auto DKc, auto NXTc, const int t, const unsigned vsp, const unsigned vsn, f32x16& sp, f32x16& s) {
    constexpr bool QK = decltype(QKc)::value, PVV = decltype(PVc)::value, DV = decltype(DVc)::value, DK = decltype(DKc)::value, NXT = decltype(NXTc)::value;
    unsigned ka = kaddr + (t & 1) * SLOT, va0 = vaddr + vsp;
    asm volatile("" : "+v"(ka), "+v"(va0));
    u32x4 pw[2];
    const float nm = -m;
    float ps0 = 0.f, ps1 = 0.f;
    if constexpr (QK) {
#pragma unroll
      for (int r = 0; r < 16; ++r) s[r] = 0.f;
    }
    SB();
#pragma unroll
    for (int g = 0; g < 8; ++g) {
      if constexpr (QK) {
        s = __builtin_amdgcn_mfma_f32_32x32x16_bf16(kf[g], qf[g], s, 0, 0, 0);
        if (g + 3 < 8) kread(g + 3, ka);
      }
      if constexpr (PVV) {
        float e0 = __builtin_amdgcn_exp2f(fmaf(sp[2 * g], CSC, nm)), e1 = __builtin_amdgcn_exp2f(fmaf(sp[2 * g + 1], CSC, nm));
        PIN(e0); PIN(e1);
        sp[2 * g] = e0; sp[2 * g + 1] = e1;
        if (g > 0) { unsigned w = cvtpk(sp[2 * g - 2], sp[2 * g - 1]); ps0 += sp[2 * g - 2]; ps1 += sp[2 * g - 1]; PIN(w); PIN(ps0); PIN(ps1); pw[(g - 1) >> 2][(g - 1) & 3] = w; }
      }
      SB();
      if constexpr (DV) { if (g == 1 || g == 4 || g == 7) { pieceV(t, vsn, g / 3); SB(); } }
    }
    if constexpr (PVV) { unsigned w = cvtpk(sp[14], sp[15]); PIN(w); pw[1][3] = w; l += (ps0 + sp[14]) + (ps1 + sp[15]); PIN(l); }
    if constexpr (QK) {
      if constexpr (NXT) WAITVM2(3, 2, rg); else WAITVM(0);
      BARRIER();
    }
    s16x8 vf[8];
    auto vread = [&](int i) {
      const unsigned va = (va0 ^ (64u * (i & 3))) + (i >> 2) * 4096u;
      const s16x4 lo = __builtin_amdgcn_ds_read_tr16_b64_v4i16((lds_s16x4_p)(uintptr_t)(va));
      const s16x4 up = __builtin_amdgcn_ds_read_tr16_b64_v4i16((lds_s16x4_p)(uintptr_t)(va + 2048u));
      vf[i] = __builtin_shufflevector(lo, up, 0, 1, 2, 3, 4, 5, 6, 7);
    };
    if constexpr (PVV) { vread(0); vread(1); vread(2); }
    SB();
    float mx0 = 0.f, mx1 = 0.f;
    if constexpr (QK) {
      mx0 = fmaxf(fmaxf(s[0], s[1]), s[2]); mx1 = fmaxf(fmaxf(s[3], s[4]), s[5]);
      mx0 = fmaxf(fmaxf(mx0, s[6]), s[7]); mx1 = fmaxf(fmaxf(mx1, s[8]), s[9]);
      mx0 = fmaxf(fmaxf(mx0, s[10]), s[11]); mx1 = fmaxf(fmaxf(mx1, s[12]), s[13]);
      mx0 = fmaxf(fmaxf(mx0, s[14]), s[15]); mx0 = fmaxf(mx0, mx1);
      mx0 = swap_max(mx0) * CSC; PIN(mx0);
    }
    SB();
    unsigned kn = kaddr + ((t + 1) & 1) * SLOT;
    asm volatile("" : "+v"(kn));
#pragma unroll
    for (int g = 0; g < 8; ++g) {
      if constexpr (PVV) {
        o[g & 3] = __builtin_amdgcn_mfma_f32_32x32x16_bf16(vf[g], __builtin_bit_cast(s16x8, pw[g >> 2]), o[g & 3], 0, 0, 0);
        if (g + 3 < 8) vread(g + 3);
      }
      if constexpr (QK && NXT) { if (g >= 5) kread(g - 5, kn); }
      SB();
      if constexpr (DK) { if (g == 0 || g == 3 || g == 6) { pieceK(t + 2, g / 3); SB(); } }
      if constexpr (QK && NXT && !DK) { if (g == 0 || g == 3 || g == 6) { pieceV(t + 1, ((NT - 1) % 3) * SLOT, g / 3); SB(); } }
    }
    if constexpr (QK) {
      if (__builtin_expect(__any(mx0 - m > THR), 0)) {
        const float mn = fmaxf(m, mx0);
        const float alpha = __builtin_amdgcn_exp2f(m - mn);
        m = mn; l *= alpha;
#pragma unroll
        for (int i = 0; i < 4; ++i)
#pragma unroll
          for (int r = 0; r < 16; ++r) o[i][r] *= alpha;
      }
    }
  };
  {
    using T = std::true_type; using F = std::false_type;
    f32x16 sA, sB;
    WAITVM2(6, 4, rg);
    BARRIER();
    {
      const unsigned qa = lds0 + LDS_V + 2 * SLOT + rg * 8192u + r32 * 256u + 16u * ((unsigned)hi ^ (r32 & 15u));
#pragma unroll
      for (int d0 = 0; d0 < 8; ++d0) qf[d0] = *(lds_s16x8_p)(uintptr_t)(qa ^ (32u * d0));
      kread(0, kaddr); kread(1, kaddr); kread(2, kaddr);
    }
    body(T{}, F{}, F{}, T{}, T{}, 0, 0, 0, sB, sA);
    unsigned vsp = 0, vsn = SLOT;
#pragma nounroll
    for (int t = 1; t + 1 < NT - 2; t += 2) {
      body(T{}, T{}, T{}, T{}, T{}, t, vsp, vsn, sA, sB);
      vsp = vsn; vsn = vsn == 2 * SLOT ? 0 : vsn + SLOT;
      body(T{}, T{}, T{}, T{}, T{}, t + 1, vsp, vsn, sB, sA);
      vsp = vsn; vsn = vsn == 2 * SLOT ? 0 : vsn + SLOT;
    }
    body(T{}, T{}, T{}, T{}, T{}, NT - 3, ((NT - 4) % 3) * SLOT, ((NT - 3) % 3) * SLOT, sA, sB);
    body(T{}, T{}, T{}, F{}, T{}, NT - 2, ((NT - 3) % 3) * SLOT, ((NT - 2) % 3) * SLOT, sB, sA);
    body(T{}, T{}, F{}, F{}, F{}, NT - 1, ((NT - 2) % 3) * SLOT, 0, sA, sB);
    body(F{}, T{}, F{}, F{}, F{}, NT, ((NT - 1) % 3) * SLOT, 0, sB, sA);
  }

  l = swap_add(l);
  if (hi == 0) { f32x2 ml = {m, l}; *(lds_f32x2_p)(uintptr_t)(lds0 + LDS_ML + (unsigned)((rg * 4 + kg) * 32 + r32) * 8u) = ml; }
  BARRIER();
  float f;
  {
    f32x2 ml[4];
#pragma unroll
    for (int k = 0; k < 4; ++k) ml[k] = *(lds_f32x2_p)(uintptr_t)(lds0 + LDS_ML + (unsigned)((rg * 4 + k) * 32 + r32) * 8u);
    const float M = fmaxf(fmaxf(ml[0][0], ml[1][0]), fmaxf(ml[2][0], ml[3][0]));
    float L = 0.f;
#pragma unroll
    for (int k = 0; k < 4; ++k) L += ml[k][1] * __builtin_amdgcn_exp2f(ml[k][0] - M);
    f = __builtin_amdgcn_exp2f(m - M) / L;
  }
  int lane2 = lane; asm volatile("" : "+v"(lane2));
  auto fin = [&](auto KGc) {
    constexpr int KG = decltype(KGc)::value;
#pragma unroll
    for (int db = 0; db < 4; ++db) {
      if (db != KG) {
        constexpr unsigned dummy = 0; (void)dummy;
        const unsigned src = (unsigned)(KG - db - 1) & 3u;
        const unsigned base = lds0 + LDS_OX + ((unsigned)(rg * 4 + db) * 3u + src) * 4096u + lane2 * 16u;
#pragma unroll
        for (int g = 0; g < 4; ++g) {
          f32x4 w = {o[db][4 * g] * f, o[db][4 * g + 1] * f, o[db][4 * g + 2] * f, o[db][4 * g + 3] * f};
          *(lds_f32x4_p)(uintptr_t)(base + g * 1024u) = w;
        }
      }
    }
    BARRIER();
    f32x16 acc;
#pragma unroll
    for (int r = 0; r < 16; ++r) acc[r] = o[KG][r] * f;
#pragma unroll
    for (int src = 0; src < 3; ++src) {
      const unsigned base = lds0 + LDS_OX + ((unsigned)(rg * 4 + KG) * 3u + src) * 4096u + lane2 * 16u;
#pragma unroll
      for (int g = 0; g < 4; ++g) {
        const f32x4 w = *(lds_f32x4_p)(uintptr_t)(base + g * 1024u);
        acc[4 * g] += w[0]; acc[4 * g + 1] += w[1]; acc[4 * g + 2] += w[2]; acc[4 * g + 3] += w[3];
      }
    }
    char* op = (char*)O + ((size_t)bq * SQ + rg * 32 + (lane2 & 31)) * (D * 2) + KG * 64 + (lane2 >> 5) * 8;
#pragma unroll
    for (int g = 0; g < 4; ++g) {
      u32x2 w = {cvtpk(acc[4 * g], acc[4 * g + 1]), cvtpk(acc[4 * g + 2], acc[4 * g + 3])};
      *(u32x2*)(op + g * 16) = w;
    }
  };
  if (kg == 0) fin(std::integral_constant<int, 0>{});
  else if (kg == 1) fin(std::integral_constant<int, 1>{});
  else if (kg == 2) fin(std::integral_constant<int, 2>{});
  else fin(std::integral_constant<int, 3>{});
}
}

extern "C" void kernel_launch(void* const* d_in, const int* in_sizes, int n_in,
                              void* d_out, int out_size, void* d_ws, size_t ws_size,
                              hipStream_t stream) {
  (void)in_sizes; (void)n_in; (void)out_size; (void)d_ws; (void)ws_size;
  (void)hipFuncSetAttribute((const void*)gqa_fwd, hipFuncAttributeMaxDynamicSharedMemorySize, LDS_BYTES);
  hipLaunchKernelGGL(gqa_fwd, dim3(NB * HQ), dim3(NTHR), LDS_BYTES, stream,
                     (const __hip_bfloat16*)d_in[0], (const __hip_bfloat16*)d_in[1], (const __hip_bfloat16*)d_in[2], (__hip_bfloat16*)d_out);
}
```

```cpp
#include <hip/hip_runtime.h>
#include <hip/hip_bf16.h>
#include <stdint.h>
#include <type_traits>

namespace {
constexpr int NB = 8, HQ = 32, HKV = 16, SQ = 96, SK = 2048, D = 128;
constexpr int NWAVES = 12, NTHR = NWAVES * 64;
constexpr int TK = 128, NT = SK / TK;
constexpr int SLOT = TK * D * 2;
constexpr int LDS_K = 0, LDS_V = 2 * SLOT;
constexpr int LDS_ML = 0;
constexpr int LDS_OX = 16384;
constexpr int LDS_BYTES = 5 * SLOT;
constexpr float CSC = 0.088388347648318447f * 1.4426950408889634f;
constexpr float THR = 8.0f;

typedef float f32x16 __attribute__((ext_vector_type(16)));
typedef float f32x4 __attribute__((ext_vector_type(4)));
typedef float f32x2 __attribute__((ext_vector_type(2)));
typedef short s16x8 __attribute__((ext_vector_type(8)));
typedef short s16x4 __attribute__((ext_vector_type(4)));
typedef __bf16 bf16x2 __attribute__((ext_vector_type(2)));
typedef unsigned u32x4 __attribute__((ext_vector_type(4)));
typedef unsigned u32x2 __attribute__((ext_vector_type(2)));
typedef __attribute__((address_space(3))) s16x4* lds_s16x4_p;
typedef __attribute__((address_space(3))) s16x8* lds_s16x8_p;
typedef __attribute__((address_space(3))) f32x4* lds_f32x4_p;
typedef __attribute__((address_space(3))) f32x2* lds_f32x2_p;

#define DI __device__ __forceinline__
DI unsigned cvtpk(float lo, float hi) { f32x2 v = {lo, hi}; bf16x2 b = __builtin_convertvector(v, bf16x2); return __builtin_bit_cast(unsigned, b); }
DI float swap_max(float x) { auto r = __builtin_amdgcn_permlane32_swap(__float_as_uint(x), __float_as_uint(x), false, false); return fmaxf(__uint_as_float(r[0]), __uint_as_float(r[1])); }
DI float swap_add(float x) { auto r = __builtin_amdgcn_permlane32_swap(__float_as_uint(x), __float_as_uint(x), false, false); return __uint_as_float(r[0]) + __uint_as_float(r[1]); }

DI void dma1(unsigned m0v, unsigned voff, const void* sbase) {
  asm volatile("s_mov_b32 m0, %0\n\ts_nop 0\n\tglobal_load_lds_dwordx4 %1, %2 nt" :: "s"(m0v), "v"(voff), "s"(sbase) : "memory");
}
DI void dma1_skip2(unsigned m0v, unsigned voff, const void* sbase, int sel) {
  asm volatile("s_cmp_eq_u32 %3, 2\n\ts_cbranch_scc1 1f\n\ts_mov_b32 m0, %0\n\ts_nop 0\n\tglobal_load_lds_dwordx4 %1, %2 nt\n1:"
               :: "s"(m0v), "v"(voff), "s"(sbase), "s"(sel) : "memory", "scc");
}
#define WAITVM(n) asm volatile("s_waitcnt vmcnt(" #n ")" ::: "memory")
#define WAITVM2(a, b, sel) asm volatile("s_cmp_eq_u32 %0, 2\n\ts_cbranch_scc1 1f\n\ts_waitcnt vmcnt(" #a ")\n\ts_branch 2f\n1:\ts_waitcnt vmcnt(" #b ")\n2:" :: "s"(sel) : "memory", "scc")
#define PIN(x) asm volatile("" : "+v"(x))
#define BARRIER() asm volatile("s_waitcnt lgkmcnt(0)\n\ts_barrier" ::: "memory")

__global__ __launch_bounds__(NTHR, 3)
void gqa_fwd(const __hip_bfloat16* __restrict__ Q, const __hip_bfloat16* __restrict__ K, const __hip_bfloat16* __restrict__ V,
             __hip_bfloat16* __restrict__ O) {
  extern __shared__ __attribute__((aligned(16))) char shm[];
  const unsigned lds0 = (unsigned)(uintptr_t)(__attribute__((address_space(3))) char*)shm;
  const int tid = threadIdx.x, lane = tid & 63;
  const int wid = __builtin_amdgcn_readfirstlane(tid >> 6);
  const int rg = wid >> 2, kg = wid & 3;
  const int r32 = lane & 31, hi = lane >> 5;
  const int bid = blockIdx.x, xcd = bid & 7, slotid = bid >> 3;
  const int pair = xcd * 16 + (slotid >> 1), member = slotid & 1;
  const int bq = pair * 2 + member;
  const char* Kh = (const char*)K + (size_t)pair * SK * D * 2;
  const char* Vh = (const char*)V + (size_t)pair * SK * D * 2;

  const unsigned lrow = lane >> 4, lch = lane & 15;
  const unsigned ksrc = lrow * 256u + 16u * (lch ^ lrow);
  const unsigned vsrc = lrow * 256u + 16u * (lch ^ (lrow << 2));
  const unsigned pofs = kg * 8192u + rg * 1024u;
  auto pieceK = [&](int t, int k) {
      const unsigned d = lds0 + LDS_K + (t & 1) * SLOT + pofs + 3072u * k, v = ksrc ^ (64u * ((rg + 3 * k) & 3)); const char* g = Kh + (size_t)t * SLOT + pofs + 3072u * k;
      if (k < 2) dma1(d, v, g); else dma1_skip2(d, v, g, rg); };
  auto pieceV = [&](int t, unsigned vslot, int k) {
      const unsigned d = lds0 + LDS_V + vslot + pofs + 3072u * k; const char* g = Vh + (size_t)t * SLOT + pofs + 3072u * k;
      if (k < 2) dma1(d, vsrc, g); else dma1_skip2(d, vsrc, g, rg); };
  {
    const char* qg = (const char*)Q + ((size_t)bq * SQ + rg * 32) * (D * 2) + kg * 2048;
    const unsigned qd = lds0 + LDS_V + 2 * SLOT + rg * 8192u + kg * 2048u;
    dma1(qd, ksrc ^ (64u * ((2 * kg) & 3)), qg);
    dma1(qd + 1024u, ksrc ^ (64u * ((2 * kg + 1) & 3)), qg + 1024);
  }
#pragma unroll
  for (int k = 0; k < 3; ++k) pieceK(0, k);
  asm volatile("s_barrier" ::: "memory");
#pragma unroll
  for (int k = 0; k < 3; ++k) pieceK(1, k);
#pragma unroll
  for (int k = 0; k < 3; ++k) pieceV(0, 0, k);
  s16x8 qf[8];

  const unsigned kaddr = lds0 + LDS_K + (kg * 32 + r32) * 256u + 16u * ((unsigned)hi ^ (r32 & 15u));
  const unsigned tq = (lane >> 2) & 3, tg = (lane >> 4) & 1, tp = lane & 3;
  const unsigned vaddr = lds0 + LDS_V + kg * 8192u + (4u * hi + tq) * 256u + 64u * tq + 32u * tg + 8u * tp;

  f32x16 o[4];
#pragma unroll
  for (int i = 0; i < 4; ++i)
#pragma unroll
    for (int r = 0; r < 16; ++r) o[i][r] = 0.f;
  float m = -__builtin_inff(), l = 0.f;

#define SB() __builtin_amdgcn_sched_barrier(0)
  s16x8 kf[8];
  auto kread = [&](int i, unsigned ka) { kf[i] = *(lds_s16x8_p)(uintptr_t)(ka ^ (32u * i)); };
  auto body = [&](auto QKc, auto PVc, auto DVc, auto DKc, auto NXTc, const int t, const unsigned vsp, const unsigned vsn, f32x16& sp, f32x16& s) {
    constexpr bool QK = decltype(QKc)::value, PVV = decltype(PVc)::value, DV = decltype(DVc)::value, DK = decltype(DKc)::value, NXT = decltype(NXTc)::value;
    unsigned ka = kaddr + (t & 1) * SLOT, va0 = vaddr + vsp;
    asm volatile("" : "+v"(ka), "+v"(va0));
    u32x4 pw[2];
    const float nm = -m;
    float ps0 = 0.f, ps1 = 0.f;
    auto expp = [&](int k) { float e0 = __builtin_amdgcn_exp2f(fmaf(sp[2 * k], CSC, nm)), e1 = __builtin_amdgcn_exp2f(fmaf(sp[2 * k + 1], CSC, nm));
        PIN(e0); PIN(e1); sp[2 * k] = e0; sp[2 * k + 1] = e1; };
    auto packp = [&](int k) { unsigned w = cvtpk(sp[2 * k], sp[2 * k + 1]); ps0 += sp[2 * k]; ps1 += sp[2 * k + 1]; PIN(w); PIN(ps0); PIN(ps1); pw[k >> 2][k & 3] = w; };
    if constexpr (QK) {
#pragma unroll
      for (int r = 0; r < 16; ++r) s[r] = 0.f;
    }
    __builtin_amdgcn_s_setprio(1);
    SB();
#pragma unroll
    for (int g = 0; g < 8; ++g) {
      if (g == 4) __builtin_amdgcn_s_setprio(0);
      if constexpr (QK) {
        s = __builtin_amdgcn_mfma_f32_32x32x16_bf16(kf[g], qf[g], s, 0, 0, 0);
        if (g + 3 < 8) kread(g + 3, ka);
      }
      if constexpr (PVV) {
        if (g & 1) packp(g >> 1); else expp(g >> 1);
      }
      SB();
      if constexpr (DV) { if (g == 1 || g == 4 || g == 7) { pieceV(t, vsn, g / 3); SB(); } }
    }
    if constexpr (QK) {
      if constexpr (NXT) WAITVM2(3, 2, rg); else WAITVM(0);
      BARRIER();
    }
    __builtin_amdgcn_s_setprio(3);
    s16x8 vf[8];
    auto vread = [&](int i) {
      const unsigned va = (va0 ^ (64u * (i & 3))) + (i >> 2) * 4096u;
      const s16x4 lo = __builtin_amdgcn_ds_read_tr16_b64_v4i16((lds_s16x4_p)(uintptr_t)(va));
      const s16x4 up = __builtin_amdgcn_ds_read_tr16_b64_v4i16((lds_s16x4_p)(uintptr_t)(va + 2048u));
      vf[i] = __builtin_shufflevector(lo, up, 0, 1, 2, 3, 4, 5, 6, 7);
    };
    if constexpr (PVV) { vread(0); vread(1); vread(2); expp(4); }
    SB();
    float mx0 = 0.f, mx1 = 0.f;
    if constexpr (QK) {
      mx0 = fmaxf(fmaxf(s[0], s[1]), s[2]); mx1 = fmaxf(fmaxf(s[3], s[4]), s[5]);
      mx0 = fmaxf(fmaxf(mx0, s[6]), s[7]); mx1 = fmaxf(fmaxf(mx1, s[8]), s[9]);
      mx0 = fmaxf(fmaxf(mx0, s[10]), s[11]); mx1 = fmaxf(fmaxf(mx1, s[12]), s[13]);
      mx0 = fmaxf(fmaxf(mx0, s[14]), s[15]); mx0 = fmaxf(mx0, mx1);
      mx0 = swap_max(mx0) * CSC; PIN(mx0);
    }
    SB();
    unsigned kn = kaddr + ((t + 1) & 1) * SLOT;
    asm volatile("" : "+v"(kn));
#pragma unroll
    for (int g = 0; g < 8; ++g) {
      if (g == 4) __builtin_amdgcn_s_setprio(2);
      if constexpr (PVV) {
        o[g & 3] = __builtin_amdgcn_mfma_f32_32x32x16_bf16(vf[g], __builtin_bit_cast(s16x8, pw[g >> 2]), o[g & 3], 0, 0, 0);
        if (g + 3 < 8) vread(g + 3);
        if (g >= 1 && g <= 3) packp(4 + g);
        if (g == 0) packp(4);
        if (g <= 2) expp(5 + g);
        if (g == 4) { l += ps0 + ps1; PIN(l); }
      }
      if constexpr (QK && NXT) { if (g >= 5) kread(g - 5, kn); }
      SB();
      if constexpr (DK) { if (g == 0 || g == 3 || g == 6) { pieceK(t + 2, g / 3); SB(); } }
      if constexpr (QK && NXT && !DK) { if (g == 0 || g == 3 || g == 6) { pieceV(t + 1, ((NT - 1) % 3) * SLOT, g / 3); SB(); } }
    }
    if constexpr (QK) {
      if (__builtin_expect(__any(mx0 - m > THR), 0)) {
        const float mn = fmaxf(m, mx0);
        const float alpha = __builtin_amdgcn_exp2f(m - mn);
        m = mn; l *= alpha;
#pragma unroll
        for (int i = 0; i < 4; ++i)
#pragma unroll
          for (int r = 0; r < 16; ++r) o[i][r] *= alpha;
      }
    }
  };
  {
    using T = std::true_type; using F = std::false_type;
    f32x16 sA, sB;
    WAITVM2(6, 4, rg);
    BARRIER();
    {
      const unsigned qa = lds0 + LDS_V + 2 * SLOT + rg * 8192u + r32 * 256u + 16u * ((unsigned)hi ^ (r32 & 15u));
#pragma unroll
      for (int d0 = 0; d0 < 8; ++d0) qf[d0] = *(lds_s16x8_p)(uintptr_t)(qa ^ (32u * d0));
      kread(0, kaddr); kread(1, kaddr); kread(2, kaddr);
    }
    body(T{}, F{}, F{}, T{}, T{}, 0, 0, 0, sB, sA);
    unsigned vsp = 0, vsn = SLOT;
#pragma nounroll
    for (int t = 1; t + 1 < NT - 2; t += 2) {
      body(T{}, T{}, T{}, T{}, T{}, t, vsp, vsn, sA, sB);
      vsp = vsn; vsn = vsn == 2 * SLOT ? 0 : vsn + SLOT;
      body(T{}, T{}, T{}, T{}, T{}, t + 1, vsp, vsn, sB, sA);
      vsp = vsn; vsn = vsn == 2 * SLOT ? 0 : vsn + SLOT;
    }
    body(T{}, T{}, T{}, T{}, T{}, NT - 3, ((NT - 4) % 3) * SLOT, ((NT - 3) % 3) * SLOT, sA, sB);
    body(T{}, T{}, T{}, F{}, T{}, NT - 2, ((NT - 3) % 3) * SLOT, ((NT - 2) % 3) * SLOT, sB, sA);
    body(T{}, T{}, F{}, F{}, F{}, NT - 1, ((NT - 2) % 3) * SLOT, 0, sA, sB);
    body(F{}, T{}, F{}, F{}, F{}, NT, ((NT - 1) % 3) * SLOT, 0, sB, sA);
  }

  __builtin_amdgcn_s_setprio(0);
  int lane2 = lane; asm volatile("" : "+v"(lane2));
  const int q32 = lane2 & 31;
  l = swap_add(l);
  if (lane2 < 32) { f32x2 ml = {m, l}; *(lds_f32x2_p)(uintptr_t)(lds0 + LDS_ML + (unsigned)((rg * 4 + kg) * 32 + q32) * 8u) = ml; }
  auto xaddr = [&](unsigned blk) { return lds0 + 4096u + blk * 2048u + (blk >= 30u ? (unsigned)(LDS_V + SLOT - 4096 - 30 * 2048) : 0u) + lane2 * 16u; };
  auto fin = [&](auto KGc) {
    constexpr int KG = decltype(KGc)::value;
#pragma unroll
    for (int db = 0; db < 4; ++db) {
      if (db != KG) {
        const unsigned src = (unsigned)(KG - db - 1) & 3u;
        const unsigned base = xaddr((unsigned)(rg * 4 + db) * 3u + src);
#pragma unroll
        for (int h2 = 0; h2 < 2; ++h2) {
          u32x4 w = {cvtpk(o[db][8 * h2], o[db][8 * h2 + 1]), cvtpk(o[db][8 * h2 + 2], o[db][8 * h2 + 3]), cvtpk(o[db][8 * h2 + 4], o[db][8 * h2 + 5]), cvtpk(o[db][8 * h2 + 6], o[db][8 * h2 + 7])};
          *(__attribute__((address_space(3))) u32x4*)(uintptr_t)(base + h2 * 1024u) = w;
        }
      }
    }
    BARRIER();
    f32x2 ml[4];
#pragma unroll
    for (int k = 0; k < 4; ++k) ml[k] = *(lds_f32x2_p)(uintptr_t)(lds0 + LDS_ML + (unsigned)((rg * 4 + k) * 32 + q32) * 8u);
    u32x4 w[6];
#pragma unroll
    for (int src = 0; src < 3; ++src) {
      const unsigned base = xaddr((unsigned)(rg * 4 + KG) * 3u + src);
      w[2 * src] = *(__attribute__((address_space(3))) u32x4*)(uintptr_t)(base);
      w[2 * src + 1] = *(__attribute__((address_space(3))) u32x4*)(uintptr_t)(base + 1024u);
    }
    const float M = fmaxf(fmaxf(ml[0][0], ml[1][0]), fmaxf(ml[2][0], ml[3][0]));
    float fk[4], L = 0.f;
#pragma unroll
    for (int k = 0; k < 4; ++k) { fk[k] = __builtin_amdgcn_exp2f(ml[k][0] - M); L += ml[k][1] * fk[k]; }
    const float inv = 1.0f / L;
    f32x16 acc;
#pragma unroll
    for (int r = 0; r < 16; ++r) acc[r] = o[KG][r] * (fk[KG] * inv);
#pragma unroll
    for (int src = 0; src < 3; ++src) {
      const float c = fk[(KG + 1 + src) & 3] * inv;
#pragma unroll
      for (int e = 0; e < 8; ++e) {
        const unsigned x = w[2 * src + (e >> 2)][e & 3];
        acc[2 * e] += __uint_as_float(x << 16) * c; acc[2 * e + 1] += __uint_as_float(x & 0xffff0000u) * c;
      }
    }
    char* op = (char*)O + ((size_t)bq * SQ + rg * 32 + q32) * (D * 2) + KG * 64 + (lane2 >> 5) * 16;
#pragma unroll
    for (int g = 0; g < 4; g += 2) {
      unsigned a0 = cvtpk(acc[4 * g], acc[4 * g + 1]), a1 = cvtpk(acc[4 * g + 2], acc[4 * g + 3]);
      unsigned b0 = cvtpk(acc[4 * g + 4], acc[4 * g + 5]), b1 = cvtpk(acc[4 * g + 6], acc[4 * g + 7]);
      auto r0 = __builtin_amdgcn_permlane32_swap(a0, b0, false, false);
      auto r1 = __builtin_amdgcn_permlane32_swap(a1, b1, false, false);
      u32x4 wv = {r0[0], r1[0], r0[1], r1[1]};
      *(u32x4*)(op + g * 16) = wv;
    }
  };
  if (kg == 0) fin(std::integral_constant<int, 0>{});
  else if (kg == 1) fin(std::integral_constant<int, 1>{});
  else if (kg == 2) fin(std::integral_constant<int, 2>{});
  else fin(std::integral_constant<int, 3>{});
}
}

extern "C" void kernel_launch(void* const* d_in, const int* in_sizes, int n_in,
                              void* d_out, int out_size, void* d_ws, size_t ws_size,
                              hipStream_t stream) {
  (void)in_sizes; (void)n_in; (void)out_size; (void)d_ws; (void)ws_size;
  (void)hipFuncSetAttribute((const void*)gqa_fwd, hipFuncAttributeMaxDynamicSharedMemorySize, LDS_BYTES);
  hipLaunchKernelGGL(gqa_fwd, dim3(NB * HQ), dim3(NTHR), LDS_BYTES, stream,
                     (const __hip_bfloat16*)d_in[0], (const __hip_bfloat16*)d_in[1], (const __hip_bfloat16*)d_in[2], (__hip_bfloat16*)d_out);
}
```

```cpp
#include <hip/hip_runtime.h>
#include <hip/hip_bf16.h>
#include <stdint.h>
#include <type_traits>

namespace {
constexpr int NB = 8, HQ = 32, HKV = 16, SQ = 96, SK = 2048, D = 128;
constexpr int NWAVES = 12, NTHR = NWAVES * 64;
constexpr int TK = 128, NT = SK / TK;
constexpr int SLOT = TK * D * 2;
constexpr int LDS_K = 0, LDS_V = 2 * SLOT;
constexpr int LDS_ML = 0;
constexpr int LDS_OX = 16384;
constexpr int LDS_BYTES = 5 * SLOT;
constexpr float CSC = 0.088388347648318447f * 1.4426950408889634f;
constexpr float THR = 8.0f;

typedef float f32x16 __attribute__((ext_vector_type(16)));
typedef float f32x4 __attribute__((ext_vector_type(4)));
typedef float f32x2 __attribute__((ext_vector_type(2)));
typedef short s16x8 __attribute__((ext_vector_type(8)));
typedef short s16x4 __attribute__((ext_vector_type(4)));
typedef __bf16 bf16x2 __attribute__((ext_vector_type(2)));
typedef unsigned u32x4 __attribute__((ext_vector_type(4)));
typedef unsigned u32x2 __attribute__((ext_vector_type(2)));
typedef __attribute__((address_space(3))) s16x4* lds_s16x4_p;
typedef __attribute__((address_space(3))) s16x8* lds_s16x8_p;
typedef __attribute__((address_space(3))) f32x4* lds_f32x4_p;
typedef __attribute__((address_space(3))) f32x2* lds_f32x2_p;

#define DI __device__ __forceinline__
DI unsigned cvtpk(float lo, float hi) { f32x2 v = {lo, hi}; bf16x2 b = __builtin_convertvector(v, bf16x2); return __builtin_bit_cast(unsigned, b); }
DI float swap_max(float x) { auto r = __builtin_amdgcn_permlane32_swap(__float_as_uint(x), __float_as_uint(x), false, false); return fmaxf(__uint_as_float(r[0]), __uint_as_float(r[1])); }
DI float swap_add(float x) { auto r = __builtin_amdgcn_permlane32_swap(__float_as_uint(x), __float_as_uint(x), false, false); return __uint_as_float(r[0]) + __uint_as_float(r[1]); }

DI void dma1(unsigned m0v, unsigned voff, const void* sbase) {
  asm volatile("s_mov_b32 m0, %0\n\ts_nop 0\n\tglobal_load_lds_dwordx4 %1, %2 nt" :: "s"(m0v), "v"(voff), "s"(sbase) : "memory");
}
DI void dma1_skip2(unsigned m0v, unsigned voff, const void* sbase, int sel) {
  asm volatile("s_cmp_eq_u32 %3, 2\n\ts_cbranch_scc1 1f\n\ts_mov_b32 m0, %0\n\ts_nop 0\n\tglobal_load_lds_dwordx4 %1, %2 nt\n1:"
               :: "s"(m0v), "v"(voff), "s"(sbase), "s"(sel) : "memory", "scc");
}
#define WAITVM(n) asm volatile("s_waitcnt vmcnt(" #n ")" ::: "memory")
#define WAITVM2(a, b, sel) asm volatile("s_cmp_eq_u32 %0, 2\n\ts_cbranch_scc1 1f\n\ts_waitcnt vmcnt(" #a ")\n\ts_branch 2f\n1:\ts_waitcnt vmcnt(" #b ")\n2:" :: "s"(sel) : "memory", "scc")
#define PIN(x) asm volatile("" : "+v"(x))
#define BARRIER() asm volatile("s_waitcnt lgkmcnt(0)\n\ts_barrier" ::: "memory")

__global__ __launch_bounds__(NTHR, 3)
void gqa_fwd(const __hip_bfloat16* __restrict__ Q, const __hip_bfloat16* __restrict__ K, const __hip_bfloat16* __restrict__ V,
             __hip_bfloat16* __restrict__ O) {
  extern __shared__ __attribute__((aligned(16))) char shm[];
  const unsigned lds0 = (unsigned)(uintptr_t)(__attribute__((address_space(3))) char*)shm;
  const int tid = threadIdx.x, lane = tid & 63;
  const int wid = __builtin_amdgcn_readfirstlane(tid >> 6);
  const int rg = wid >> 2, kg = wid & 3;
  const int r32 = lane & 31, hi = lane >> 5;
  const int bid = blockIdx.x, xcd = bid & 7, slotid = bid >> 3;
  const int pair = xcd * 16 + (slotid >> 1), member = slotid & 1;
  const int bq = pair * 2 + member;
  const char* Kh = (const char*)K + (size_t)pair * SK * D * 2;
  const char* Vh = (const char*)V + (size_t)pair * SK * D * 2;

  const unsigned lrow = lane >> 4, lch = lane & 15;
  const unsigned ksrc = lrow * 256u + 16u * (lch ^ lrow);
  const unsigned vsrc = lrow * 256u + 16u * (lch ^ (lrow << 2));
  const unsigned pofs = kg * 8192u + rg * 1024u;
  auto pieceK = [&](int t, int k) {
      const unsigned d = lds0 + LDS_K + (t & 1) * SLOT + pofs + 3072u * k, v = ksrc ^ (64u * ((rg + 3 * k) & 3)); const char* g = Kh + (size_t)t * SLOT + pofs + 3072u * k;
      if (k < 2) dma1(d, v, g); else dma1_skip2(d, v, g, rg); };
  auto pieceV = [&](int t, unsigned vslot, int k) {
      const unsigned d = lds0 + LDS_V + vslot + pofs + 3072u * k; const char* g = Vh + (size_t)t * SLOT + pofs + 3072u * k;
      if (k < 2) dma1(d, vsrc, g); else dma1_skip2(d, vsrc, g, rg); };
  {
    const char* qg = (const char*)Q + ((size_t)bq * SQ + rg * 32) * (D * 2) + kg * 2048;
    const unsigned qd = lds0 + LDS_V + 2 * SLOT + rg * 8192u + kg * 2048u;
    dma1(qd, ksrc ^ (64u * ((2 * kg) & 3)), qg);
    dma1(qd + 1024u, ksrc ^ (64u * ((2 * kg + 1) & 3)), qg + 1024);
  }
#pragma unroll
  for (int k = 0; k < 3; ++k) pieceK(0, k);
  asm volatile("s_barrier" ::: "memory");
#pragma unroll
  for (int k = 0; k < 3; ++k) pieceK(1, k);
#pragma unroll
  for (int k = 0; k < 3; ++k) pieceV(0, 0, k);
  s16x8 qf[8];

  const unsigned kaddr = lds0 + LDS_K + (kg * 32 + r32) * 256u + 16u * ((unsigned)hi ^ (r32 & 15u));
  const unsigned tq = (lane >> 2) & 3, tg = (lane >> 4) & 1, tp = lane & 3;
  const unsigned vaddr = lds0 + LDS_V + kg * 8192u + (4u * hi + tq) * 256u + 64u * tq + 32u * tg + 8u * tp;

  f32x16 o[4];
#pragma unroll
  for (int i = 0; i < 4; ++i)
#pragma unroll
    for (int r = 0; r < 16; ++r) o[i][r] = 0.f;
  float m = -__builtin_inff(), l = 0.f;

#define SB() __builtin_amdgcn_sched_barrier(0)
  s16x8 kf[8];
  auto kread = [&](int i, unsigned ka) { kf[i] = *(lds_s16x8_p)(uintptr_t)(ka ^ (32u * i)); };
  auto body = [&](auto QKc, auto PVc, auto DVc, auto DKc, auto NXTc, const int t, const unsigned vsp, const unsigned vsn, f32x16& sp, f32x16& s) {
    constexpr bool QK = decltype(QKc)::value, PVV = decltype(PVc)::value, DV = decltype(DVc)::value, DK = decltype(DKc)::value, NXT = decltype(NXTc)::value;
    unsigned ka = kaddr + (t & 1) * SLOT, va0 = vaddr + vsp;
    asm volatile("" : "+v"(ka), "+v"(va0));
    u32x4 pw[2];
    const float nm = -m;
    float ps0 = 0.f, ps1 = 0.f;
    auto expp = [&](int k) { float e0 = __builtin_amdgcn_exp2f(fmaf(sp[2 * k], CSC, nm)), e1 = __builtin_amdgcn_exp2f(fmaf(sp[2 * k + 1], CSC, nm));
        PIN(e0); PIN(e1); sp[2 * k] = e0; sp[2 * k + 1] = e1; };
    auto packp = [&](int k) { unsigned w = cvtpk(sp[2 * k], sp[2 * k + 1]); ps0 += sp[2 * k]; ps1 += sp[2 * k + 1]; PIN(w); PIN(ps0); PIN(ps1); pw[k >> 2][k & 3] = w; };
    if constexpr (QK) {
#pragma unroll
      for (int r = 0; r < 16; ++r) s[r] = 0.f;
    }
    __builtin_amdgcn_s_setprio(1);
    SB();
#pragma unroll
    for (int g = 0; g < 8; ++g) {
      if (g == 4) __builtin_amdgcn_s_setprio(0);
      if constexpr (QK) {
        s = __builtin_amdgcn_mfma_f32_32x32x16_bf16(kf[g], qf[g], s, 0, 0, 0);
        if (g + 3 < 8) kread(g + 3, ka);
      }
      if constexpr (PVV) {
        if (g & 1) packp(g >> 1); else expp(g >> 1);
      }
      SB();
      if constexpr (DV) { if (g == 1 || g == 4 || g == 7) { pieceV(t, vsn, g / 3); SB(); } }
    }
    if constexpr (QK) {
      if constexpr (NXT) WAITVM2(6, 4, rg); else WAITVM(0);
      BARRIER();
    }
    __builtin_amdgcn_s_setprio(3);
    s16x8 vf[8];
    auto vread = [&](int i) {
      const unsigned va = (va0 ^ (64u * (i & 3))) + (i >> 2) * 4096u;
      const s16x4 lo = __builtin_amdgcn_ds_read_tr16_b64_v4i16((lds_s16x4_p)(uintptr_t)(va));
      const s16x4 up = __builtin_amdgcn_ds_read_tr16_b64_v4i16((lds_s16x4_p)(uintptr_t)(va + 2048u));
      vf[i] = __builtin_shufflevector(lo, up, 0, 1, 2, 3, 4, 5, 6, 7);
    };
    if constexpr (PVV) { vread(0); vread(1); vread(2); expp(4); }
    SB();
    float mx0 = 0.f, mx1 = 0.f;
    if constexpr (QK) {
      mx0 = fmaxf(fmaxf(s[0], s[1]), s[2]); mx1 = fmaxf(fmaxf(s[3], s[4]), s[5]);
      mx0 = fmaxf(fmaxf(mx0, s[6]), s[7]); mx1 = fmaxf(fmaxf(mx1, s[8]), s[9]);
      mx0 = fmaxf(fmaxf(mx0, s[10]), s[11]); mx1 = fmaxf(fmaxf(mx1, s[12]), s[13]);
      mx0 = fmaxf(fmaxf(mx0, s[14]), s[15]); mx0 = fmaxf(mx0, mx1);
      mx0 = swap_max(mx0) * CSC; PIN(mx0);
    }
    SB();
    unsigned kn = kaddr + ((t + 1) & 1) * SLOT;
    asm volatile("" : "+v"(kn));
#pragma unroll
    for (int g = 0; g < 8; ++g) {
      if (g == 4) __builtin_amdgcn_s_setprio(2);
      if constexpr (PVV) {
        o[g & 3] = __builtin_amdgcn_mfma_f32_32x32x16_bf16(vf[g], __builtin_bit_cast(s16x8, pw[g >> 2]), o[g & 3], 0, 0, 0);
        if (g + 3 < 8) vread(g + 3);
        if (g >= 1 && g <= 3) packp(4 + g);
        if (g == 0) packp(4);
        if (g <= 2) expp(5 + g);
        if (g == 4) { l += ps0 + ps1; PIN(l); }
      }
      if constexpr (QK && NXT) { if (g >= 5) kread(g - 5, kn); }
      SB();
      if constexpr (DK) { if (g < 3) { pieceK(t + 2, g); SB(); } }
      if constexpr (QK && NXT && !DK) { if (g < 3) { pieceV(t + 1, ((NT - 1) % 3) * SLOT, g); SB(); } }
      if constexpr (QK && NXT) { if (g == 4) { WAITVM2(6, 4, rg); asm volatile("s_barrier" ::: "memory"); SB(); } }
    }
    if constexpr (QK) {
      if (__builtin_expect(__any(mx0 - m > THR), 0)) {
        const float mn = fmaxf(m, mx0);
        const float alpha = __builtin_amdgcn_exp2f(m - mn);
        m = mn; l *= alpha;
#pragma unroll
        for (int i = 0; i < 4; ++i)
#pragma unroll
          for (int r = 0; r < 16; ++r) o[i][r] *= alpha;
      }
    }
  };
  {
    using T = std::true_type; using F = std::false_type;
    f32x16 sA, sB;
    WAITVM2(6, 4, rg);
    BARRIER();
    {
      const unsigned qa = lds0 + LDS_V + 2 * SLOT + rg * 8192u + r32 * 256u + 16u * ((unsigned)hi ^ (r32 & 15u));
#pragma unroll
      for (int d0 = 0; d0 < 8; ++d0) qf[d0] = *(lds_s16x8_p)(uintptr_t)(qa ^ (32u * d0));
      kread(0, kaddr); kread(1, kaddr); kread(2, kaddr);
    }
    body(T{}, F{}, F{}, T{}, T{}, 0, 0, 0, sB, sA);
    unsigned vsp = 0, vsn = SLOT;
#pragma nounroll
    for (int t = 1; t + 1 < NT - 2; t += 2) {
      body(T{}, T{}, T{}, T{}, T{}, t, vsp, vsn, sA, sB);
      vsp = vsn; vsn = vsn == 2 * SLOT ? 0 : vsn + SLOT;
      body(T{}, T{}, T{}, T{}, T{}, t + 1, vsp, vsn, sB, sA);
      vsp = vsn; vsn = vsn == 2 * SLOT ? 0 : vsn + SLOT;
    }
    body(T{}, T{}, T{}, T{}, T{}, NT - 3, ((NT - 4) % 3) * SLOT, ((NT - 3) % 3) * SLOT, sA, sB);
    body(T{}, T{}, T{}, F{}, T{}, NT - 2, ((NT - 3) % 3) * SLOT, ((NT - 2) % 3) * SLOT, sB, sA);
    body(T{}, T{}, F{}, F{}, F{}, NT - 1, ((NT - 2) % 3) * SLOT, 0, sA, sB);
    body(F{}, T{}, F{}, F{}, F{}, NT, ((NT - 1) % 3) * SLOT, 0, sB, sA);
  }

  __builtin_amdgcn_s_setprio(0);
  int lane2 = lane; asm volatile("" : "+v"(lane2));
  const int q32 = lane2 & 31;
  l = swap_add(l);
  if (lane2 < 32) { f32x2 ml = {m, l}; *(lds_f32x2_p)(uintptr_t)(lds0 + LDS_ML + (unsigned)((rg * 4 + kg) * 32 + q32) * 8u) = ml; }
  auto xaddr = [&](unsigned blk) { return lds0 + 4096u + blk * 2048u + (blk >= 30u ? (unsigned)(LDS_V + SLOT - 4096 - 30 * 2048) : 0u) + lane2 * 16u; };
  auto fin = [&](auto KGc) {
    constexpr int KG = decltype(KGc)::value;
#pragma unroll
    for (int db = 0; db < 4; ++db) {
      if (db != KG) {
        const unsigned src = (unsigned)(KG - db - 1) & 3u;
        const unsigned base = xaddr((unsigned)(rg * 4 + db) * 3u + src);
#pragma unroll
        for (int h2 = 0; h2 < 2; ++h2) {
          u32x4 w = {cvtpk(o[db][8 * h2], o[db][8 * h2 + 1]), cvtpk(o[db][8 * h2 + 2], o[db][8 * h2 + 3]), cvtpk(o[db][8 * h2 + 4], o[db][8 * h2 + 5]), cvtpk(o[db][8 * h2 + 6], o[db][8 * h2 + 7])};
          *(__attribute__((address_space(3))) u32x4*)(uintptr_t)(base + h2 * 1024u) = w;
        }
      }
    }
    BARRIER();
    f32x2 ml[4];
#pragma unroll
    for (int k = 0; k < 4; ++k) ml[k] = *(lds_f32x2_p)(uintptr_t)(lds0 + LDS_ML + (unsigned)((rg * 4 + k) * 32 + q32) * 8u);
    u32x4 w[6];
#pragma unroll
    for (int src = 0; src < 3; ++src) {
      const unsigned base = xaddr((unsigned)(rg * 4 + KG) * 3u + src);
      w[2 * src] = *(__attribute__((address_space(3))) u32x4*)(uintptr_t)(base);
      w[2 * src + 1] = *(__attribute__((address_space(3))) u32x4*)(uintptr_t)(base + 1024u);
    }
    const float M = fmaxf(fmaxf(ml[0][0], ml[1][0]), fmaxf(ml[2][0], ml[3][0]));
    float fk[4], L = 0.f;
#pragma unroll
    for (int k = 0; k < 4; ++k) { fk[k] = __builtin_amdgcn_exp2f(ml[k][0] - M); L += ml[k][1] * fk[k]; }
    const float inv = 1.0f / L;
    f32x16 acc;
#pragma unroll
    for (int r = 0; r < 16; ++r) acc[r] = o[KG][r] * (fk[KG] * inv);
#pragma unroll
    for (int src = 0; src < 3; ++src) {
      const float c = fk[(KG + 1 + src) & 3] * inv;
#pragma unroll
      for (int e = 0; e < 8; ++e) {
        const unsigned x = w[2 * src + (e >> 2)][e & 3];
        acc[2 * e] += __uint_as_float(x << 16) * c; acc[2 * e + 1] += __uint_as_float(x & 0xffff0000u) * c;
      }
    }
    char* op = (char*)O + ((size_t)bq * SQ + rg * 32 + q32) * (D * 2) + KG * 64 + (lane2 >> 5) * 16;
#pragma unroll
    for (int g = 0; g < 4; g += 2) {
      unsigned a0 = cvtpk(acc[4 * g], acc[4 * g + 1]), a1 = cvtpk(acc[4 * g + 2], acc[4 * g + 3]);
      unsigned b0 = cvtpk(acc[4 * g + 4], acc[4 * g + 5]), b1 = cvtpk(acc[4 * g + 6], acc[4 * g + 7]);
      auto r0 = __builtin_amdgcn_permlane32_swap(a0, b0, false, false);
      auto r1 = __builtin_amdgcn_permlane32_swap(a1, b1, false, false);
      u32x4 wv = {r0[0], r1[0], r0[1], r1[1]};
      *(u32x4*)(op + g * 16) = wv;
    }
  };
  if (kg == 0) fin(std::integral_constant<int, 0>{});
  else if (kg == 1) fin(std::integral_constant<int, 1>{});
  else if (kg == 2) fin(std::integral_constant<int, 2>{});
  else fin(std::integral_constant<int, 3>{});
}
}

extern "C" void kernel_launch(void* const* d_in, const int* in_sizes, int n_in,
                              void* d_out, int out_size, void* d_ws, size_t ws_size,
                              hipStream_t stream) {
  (void)in_sizes; (void)n_in; (void)out_size; (void)d_ws; (void)ws_size;
  (void)hipFuncSetAttribute((const void*)gqa_fwd, hipFuncAttributeMaxDynamicSharedMemorySize, LDS_BYTES);
  hipLaunchKernelGGL(gqa_fwd, dim3(NB * HQ), dim3(NTHR), LDS_BYTES, stream,
                     (const __hip_bfloat16*)d_in[0], (const __hip_bfloat16*)d_in[1], (const __hip_bfloat16*)d_in[2], (__hip_bfloat16*)d_out);
}
```

```cpp
#include <hip/hip_runtime.h>
#include <hip/hip_bf16.h>
#include <stdint.h>
#include <type_traits>

namespace {
constexpr int NB = 8, HQ = 32, HKV = 16, SQ = 96, SK = 2048, D = 128;
constexpr int NWAVES = 12, NTHR = NWAVES * 64;
constexpr int TK = 128, NT = SK / TK;
constexpr int SLOT = TK * D * 2;
constexpr int LDS_K = 0, LDS_V = 2 * SLOT;
constexpr int LDS_ML = 0;
constexpr int LDS_OX = 16384;
constexpr int LDS_BYTES = 5 * SLOT;
constexpr float CSC = 0.088388347648318447f * 1.4426950408889634f;
constexpr float THR = 8.0f;

typedef float f32x16 __attribute__((ext_vector_type(16)));
typedef float f32x4 __attribute__((ext_vector_type(4)));
typedef float f32x2 __attribute__((ext_vector_type(2)));
typedef short s16x8 __attribute__((ext_vector_type(8)));
typedef short s16x4 __attribute__((ext_vector_type(4)));
typedef __bf16 bf16x2 __attribute__((ext_vector_type(2)));
typedef unsigned u32x4 __attribute__((ext_vector_type(4)));
typedef unsigned u32x2 __attribute__((ext_vector_type(2)));
typedef __attribute__((address_space(3))) s16x4* lds_s16x4_p;
typedef __attribute__((address_space(3))) s16x8* lds_s16x8_p;
typedef __attribute__((address_space(3))) f32x4* lds_f32x4_p;
typedef __attribute__((address_space(3))) f32x2* lds_f32x2_p;

#define DI __device__ __forceinline__
DI unsigned cvtpk(float lo, float hi) { f32x2 v = {lo, hi}; bf16x2 b = __builtin_convertvector(v, bf16x2); return __builtin_bit_cast(unsigned, b); }
DI float swap_max(float x) { auto r = __builtin_amdgcn_permlane32_swap(__float_as_uint(x), __float_as_uint(x), false, false); return fmaxf(__uint_as_float(r[0]), __uint_as_float(r[1])); }
DI float swap16_max(float x) { auto r = __builtin_amdgcn_permlane16_swap(__float_as_uint(x), __float_as_uint(x), false, false); return fmaxf(__uint_as_float(r[0]), __uint_as_float(r[1])); }
DI float swap16_add(float x) { auto r = __builtin_amdgcn_permlane16_swap(__float_as_uint(x), __float_as_uint(x), false, false); return __uint_as_float(r[0]) + __uint_as_float(r[1]); }
#define MFMA16(a, b, c) __builtin_amdgcn_mfma_f32_16x16x32_bf16((a), (b), (c), 0, 0, 0)
DI float swap_add(float x) { auto r = __builtin_amdgcn_permlane32_swap(__float_as_uint(x), __float_as_uint(x), false, false); return __uint_as_float(r[0]) + __uint_as_float(r[1]); }

DI void dma1(unsigned m0v, unsigned voff, const void* sbase) {
  asm volatile("s_mov_b32 m0, %0\n\ts_nop 0\n\tglobal_load_lds_dwordx4 %1, %2 nt" :: "s"(m0v), "v"(voff), "s"(sbase) : "memory");
}
DI void dma1_skip2(unsigned m0v, unsigned voff, const void* sbase, int sel) {
  asm volatile("s_cmp_eq_u32 %3, 2\n\ts_cbranch_scc1 1f\n\ts_mov_b32 m0, %0\n\ts_nop 0\n\tglobal_load_lds_dwordx4 %1, %2 nt\n1:"
               :: "s"(m0v), "v"(voff), "s"(sbase), "s"(sel) : "memory", "scc");
}
#define WAITVM(n) asm volatile("s_waitcnt vmcnt(" #n ")" ::: "memory")
#define WAITVM2(a, b, sel) asm volatile("s_cmp_eq_u32 %0, 2\n\ts_cbranch_scc1 1f\n\ts_waitcnt vmcnt(" #a ")\n\ts_branch 2f\n1:\ts_waitcnt vmcnt(" #b ")\n2:" :: "s"(sel) : "memory", "scc")
#define PIN(x) asm volatile("" : "+v"(x))
#define BARRIER() asm volatile("s_waitcnt lgkmcnt(0)\n\ts_barrier" ::: "memory")

__global__ __launch_bounds__(NTHR, 3)
void gqa_fwd(const __hip_bfloat16* __restrict__ Q, const __hip_bfloat16* __restrict__ K, const __hip_bfloat16* __restrict__ V,
             __hip_bfloat16* __restrict__ O) {
  extern __shared__ __attribute__((aligned(16))) char shm[];
  const unsigned lds0 = (unsigned)(uintptr_t)(__attribute__((address_space(3))) char*)shm;
  const int tid = threadIdx.x, lane = tid & 63;
  const int wid = __builtin_amdgcn_readfirstlane(tid >> 6);
  const int rg = wid >> 2, kg = wid & 3;
  const int c16 = lane & 15, g4 = lane >> 4;
  const int bid = blockIdx.x, xcd = bid & 7, slotid = bid >> 3;
  const int pair = xcd * 16 + (slotid >> 1), member = slotid & 1;
  const int bq = pair * 2 + member;
  const char* Kh = (const char*)K + (size_t)pair * SK * D * 2;
  const char* Vh = (const char*)V + (size_t)pair * SK * D * 2;

  const unsigned lrow = lane >> 4, lch = lane & 15;
  const unsigned ksrc = lrow * 256u + 16u * (lch ^ lrow);
  const unsigned vsrc = lrow * 256u + 16u * (lch ^ (lrow << 2));
  const unsigned pofs = kg * 8192u + rg * 1024u;
  auto pieceK = [&](int t, int k) {
      const unsigned d = lds0 + LDS_K + (t & 1) * SLOT + pofs + 3072u * k, v = ksrc ^ (64u * ((rg + 3 * k) & 3)); const char* g = Kh + (size_t)t * SLOT + pofs + 3072u * k;
      if (k < 2) dma1(d, v, g); else dma1_skip2(d, v, g, rg); };
  auto pieceV = [&](int t, unsigned vslot, int k) {
      const unsigned d = lds0 + LDS_V + vslot + pofs + 3072u * k; const char* g = Vh + (size_t)t * SLOT + pofs + 3072u * k;
      const unsigned v = vsrc ^ (32u * ((rg + 3 * k) & 1));
      if (k < 2) dma1(d, v, g); else dma1_skip2(d, v, g, rg); };
  {
    const char* qg = (const char*)Q + ((size_t)bq * SQ + rg * 32) * (D * 2) + kg * 2048;
    const unsigned qd = lds0 + LDS_V + 2 * SLOT + rg * 8192u + kg * 2048u;
    dma1(qd, ksrc ^ (64u * ((2 * kg) & 3)), qg);
    dma1(qd + 1024u, ksrc ^ (64u * ((2 * kg + 1) & 3)), qg + 1024);
  }
#pragma unroll
  for (int k = 0; k < 3; ++k) pieceK(0, k);
  asm volatile("s_barrier" ::: "memory");
#pragma unroll
  for (int k = 0; k < 3; ++k) pieceK(1, k);
#pragma unroll
  for (int k = 0; k < 3; ++k) pieceV(0, 0, k);
  s16x8 qf[8];

  const unsigned kaddr = lds0 + LDS_K + (kg * 32 + c16) * 256u + 16u * ((unsigned)g4 ^ (unsigned)c16);
  const unsigned tq = (lane >> 2) & 3, tp = lane & 3;
  const unsigned vaddr = lds0 + LDS_V + kg * 8192u + (4u * g4 + tq) * 256u + 16u * ((tp >> 1) ^ (tq << 2) ^ ((g4 & 1u) << 1)) + 8u * (tp & 1);

  f32x4 o[8][2];
#pragma unroll
  for (int i = 0; i < 8; ++i) { o[i][0] = f32x4{0.f, 0.f, 0.f, 0.f}; o[i][1] = f32x4{0.f, 0.f, 0.f, 0.f}; }
  float m0 = -__builtin_inff(), m1 = -__builtin_inff(), l0 = 0.f, l1 = 0.f;

#define SB() __builtin_amdgcn_sched_barrier(0)
  struct S4 { f32x4 v[2][2]; };
  s16x8 kf[8];
  auto kread = [&](int i, unsigned ka) { kf[i] = *(lds_s16x8_p)(uintptr_t)((ka ^ (64u * (i >> 1))) + 4096u * (i & 1)); };
  auto body = [&](auto QKc, auto PVc, auto DVc, auto DKc, auto NXTc, const int t, const unsigned vsp, const unsigned vsn, S4& sp, S4& s) {
    constexpr bool QK = decltype(QKc)::value, PVV = decltype(PVc)::value, DV = decltype(DVc)::value, DK = decltype(DKc)::value, NXT = decltype(NXTc)::value;
    unsigned ka = kaddr + (t & 1) * SLOT, va0 = vaddr + vsp;
    asm volatile("" : "+v"(ka), "+v"(va0));
    u32x4 pw[2];
    const float nm[2] = {-m0, -m1};
    float ps[2] = {0.f, 0.f};
    auto expp = [&](int k) { f32x4& x = sp.v[(k >> 1) & 1][k >> 2]; const int r = 2 * (k & 1);
        float e0 = __builtin_amdgcn_exp2f(fmaf(x[r], CSC, nm[k >> 2])), e1 = __builtin_amdgcn_exp2f(fmaf(x[r + 1], CSC, nm[k >> 2]));
        PIN(e0); PIN(e1); x[r] = e0; x[r + 1] = e1; };
    auto packp = [&](int k) { f32x4& x = sp.v[(k >> 1) & 1][k >> 2]; const int r = 2 * (k & 1);
        unsigned w = cvtpk(x[r], x[r + 1]); float a = ps[k >> 2] + (x[r] + x[r + 1]); PIN(w); PIN(a); ps[k >> 2] = a; pw[k >> 2][k & 3] = w; };
    if constexpr (QK) {
#pragma unroll
      for (int i = 0; i < 4; ++i) s.v[i >> 1][i & 1] = f32x4{0.f, 0.f, 0.f, 0.f};
    }
    __builtin_amdgcn_s_setprio(1);
    SB();
#pragma unroll
    for (int g = 0; g < 8; ++g) {
      if (g == 4) __builtin_amdgcn_s_setprio(0);
      if constexpr (QK) {
        s.v[g & 1][0] = MFMA16(kf[g], qf[g & ~1], s.v[g & 1][0]);
        s.v[g & 1][1] = MFMA16(kf[g], qf[g | 1], s.v[g & 1][1]);
        if (g + 3 < 8) kread(g + 3, ka);
      }
      if constexpr (PVV) { if (g & 1) packp(g >> 1); else expp(g >> 1); }
      SB();
      if constexpr (DV) { if (g == 1 || g == 4 || g == 7) { pieceV(t, vsn, g / 3); SB(); } }
    }
    if constexpr (QK) {
      if constexpr (NXT) WAITVM2(3, 2, rg); else WAITVM(0);
      BARRIER();
    }
    __builtin_amdgcn_s_setprio(3);
    s16x8 vf[8];
    auto vread = [&](int db) {
      const unsigned va = va0 ^ (32u * db);
      const s16x4 lo = __builtin_amdgcn_ds_read_tr16_b64_v4i16((lds_s16x4_p)(uintptr_t)(va));
      const s16x4 up = __builtin_amdgcn_ds_read_tr16_b64_v4i16((lds_s16x4_p)(uintptr_t)(va + 4096u));
      vf[db] = __builtin_shufflevector(lo, up, 0, 1, 2, 3, 4, 5, 6, 7);
    };
    if constexpr (PVV) { vread(0); vread(1); vread(2); vread(3); expp(4); }
    SB();
    float mx0 = 0.f, mx1 = 0.f;
    if constexpr (QK) {
      mx0 = fmaxf(fmaxf(s.v[0][0][0], s.v[0][0][1]), s.v[0][0][2]); mx1 = fmaxf(fmaxf(s.v[0][1][0], s.v[0][1][1]), s.v[0][1][2]);
      mx0 = fmaxf(fmaxf(mx0, s.v[0][0][3]), s.v[1][0][0]); mx1 = fmaxf(fmaxf(mx1, s.v[0][1][3]), s.v[1][1][0]);
      mx0 = fmaxf(fmaxf(mx0, s.v[1][0][1]), s.v[1][0][2]); mx1 = fmaxf(fmaxf(mx1, s.v[1][1][1]), s.v[1][1][2]);
      mx0 = fmaxf(mx0, s.v[1][0][3]); mx1 = fmaxf(mx1, s.v[1][1][3]);
      PIN(mx0); PIN(mx1);
    }
    SB();
    unsigned kn = kaddr + ((t + 1) & 1) * SLOT;
    asm volatile("" : "+v"(kn));
#pragma unroll
    for (int g = 0; g < 8; ++g) {
      if (g == 4) __builtin_amdgcn_s_setprio(2);
      if constexpr (PVV) {
        const s16x8 p0 = __builtin_bit_cast(s16x8, pw[0]);
        if (g < 2) {
          o[2 * g][0] = MFMA16(vf[2 * g], p0, o[2 * g][0]); o[2 * g + 1][0] = MFMA16(vf[2 * g + 1], p0, o[2 * g + 1][0]);
          packp(4 + 2 * g); expp(5 + 2 * g); packp(5 + 2 * g); if (g == 0) expp(6);
        } else if (g < 4) {
          const s16x8 p1 = __builtin_bit_cast(s16x8, pw[1]);
          o[2 * g - 4][1] = MFMA16(vf[2 * g - 4], p1, o[2 * g - 4][1]); o[2 * g - 3][1] = MFMA16(vf[2 * g - 3], p1, o[2 * g - 3][1]);
          vread(g + 2 + (g - 2)); vread(g + 3 + (g - 2));
        } else {
          const s16x8 p1 = __builtin_bit_cast(s16x8, pw[1]);
          o[g][0] = MFMA16(vf[g], p0, o[g][0]); o[g][1] = MFMA16(vf[g], p1, o[g][1]);
        }
        if (g == 4) { l0 += ps[0]; l1 += ps[1]; PIN(l0); PIN(l1); }
      }
      if constexpr (QK) {
        if (g == 2) { mx0 = swap_max(mx0); mx1 = swap_max(mx1); PIN(mx0); PIN(mx1); }
        if (g == 3) { mx0 = swap16_max(mx0) * CSC; mx1 = swap16_max(mx1) * CSC; PIN(mx0); PIN(mx1); }
      }
      if constexpr (QK && NXT) { if (g >= 5) kread(g - 5, kn); }
      SB();
      if constexpr (DK) { if (g == 0 || g == 3 || g == 6) { pieceK(t + 2, g / 3); SB(); } }
      if constexpr (QK && NXT && !DK) { if (g == 0 || g == 3 || g == 6) { pieceV(t + 1, ((NT - 1) % 3) * SLOT, g / 3); SB(); } }
    }
    if constexpr (QK) {
      if (__builtin_expect(__any((mx0 - m0 > THR) || (mx1 - m1 > THR)), 0)) {
        const float mn0 = fmaxf(m0, mx0), mn1 = fmaxf(m1, mx1);
        const float a0 = __builtin_amdgcn_exp2f(m0 - mn0), a1 = __builtin_amdgcn_exp2f(m1 - mn1);
        m0 = mn0; m1 = mn1; l0 *= a0; l1 *= a1;
#pragma unroll
        for (int i = 0; i < 8; ++i) { o[i][0] *= a0; o[i][1] *= a1; }
      }
    }
  };
  {
    using T = std::true_type; using F = std::false_type;
    S4 sA, sB;
    WAITVM2(6, 4, rg);
    BARRIER();
    {
      const unsigned qa = lds0 + LDS_V + 2 * SLOT + rg * 8192u + c16 * 256u + 16u * ((unsigned)g4 ^ (unsigned)c16);
#pragma unroll
      for (int i = 0; i < 8; ++i) qf[i] = *(lds_s16x8_p)(uintptr_t)((qa ^ (64u * (i >> 1))) + 4096u * (i & 1));
      kread(0, kaddr); kread(1, kaddr); kread(2, kaddr);
    }
    body(T{}, F{}, F{}, T{}, T{}, 0, 0, 0, sB, sA);
    unsigned vsp = 0, vsn = SLOT;
#pragma nounroll
    for (int t = 1; t + 1 < NT - 2; t += 2) {
      body(T{}, T{}, T{}, T{}, T{}, t, vsp, vsn, sA, sB);
      vsp = vsn; vsn = vsn == 2 * SLOT ? 0 : vsn + SLOT;
      body(T{}, T{}, T{}, T{}, T{}, t + 1, vsp, vsn, sB, sA);
      vsp = vsn; vsn = vsn == 2 * SLOT ? 0 : vsn + SLOT;
    }
    body(T{}, T{}, T{}, T{}, T{}, NT - 3, ((NT - 4) % 3) * SLOT, ((NT - 3) % 3) * SLOT, sA, sB);
    body(T{}, T{}, T{}, F{}, T{}, NT - 2, ((NT - 3) % 3) * SLOT, ((NT - 2) % 3) * SLOT, sB, sA);
    body(T{}, T{}, F{}, F{}, F{}, NT - 1, ((NT - 2) % 3) * SLOT, 0, sA, sB);
    body(F{}, T{}, F{}, F{}, F{}, NT, ((NT - 1) % 3) * SLOT, 0, sB, sA);
  }

  __builtin_amdgcn_s_setprio(0);
  int lane2 = lane; asm volatile("" : "+v"(lane2));
  const int e16 = lane2 & 15, eg = lane2 >> 4;
  l0 = swap16_add(swap_add(l0)); l1 = swap16_add(swap_add(l1));
  if (lane2 < 32) { f32x2 ml = {eg ? m1 : m0, eg ? l1 : l0}; *(lds_f32x2_p)(uintptr_t)(lds0 + LDS_ML + (unsigned)((rg * 4 + kg) * 32 + lane2) * 8u) = ml; }
  auto xaddr = [&](unsigned blk) { return lds0 + 4096u + blk * 2048u + (blk >= 30u ? (unsigned)(LDS_V + SLOT - 4096 - 30 * 2048) : 0u) + lane2 * 16u; };
  auto fin = [&](auto KGc) {
    constexpr int KG = decltype(KGc)::value;
#pragma unroll
    for (int dk = 0; dk < 4; ++dk) {
      if (dk != KG) {
        const unsigned src = (unsigned)(KG - dk - 1) & 3u;
        const unsigned base = xaddr((unsigned)(rg * 4 + dk) * 3u + src);
#pragma unroll
        for (int qb = 0; qb < 2; ++qb) {
          const f32x4 x = o[2 * dk][qb], y = o[2 * dk + 1][qb];
          u32x4 w = {cvtpk(x[0], x[1]), cvtpk(x[2], x[3]), cvtpk(y[0], y[1]), cvtpk(y[2], y[3])};
          *(__attribute__((address_space(3))) u32x4*)(uintptr_t)(base + qb * 1024u) = w;
        }
      }
    }
    BARRIER();
    u32x4 w[6];
#pragma unroll
    for (int src = 0; src < 3; ++src) {
      const unsigned base = xaddr((unsigned)(rg * 4 + KG) * 3u + src);
      w[2 * src] = *(__attribute__((address_space(3))) u32x4*)(uintptr_t)(base);
      w[2 * src + 1] = *(__attribute__((address_space(3))) u32x4*)(uintptr_t)(base + 1024u);
    }
    char* op = (char*)O + ((size_t)bq * SQ + rg * 32 + e16) * (D * 2) + KG * 64 + (eg & 1) * 32 + (eg >> 1) * 16;
#pragma unroll
    for (int qb = 0; qb < 2; ++qb) {
      f32x2 ml[4];
#pragma unroll
      for (int k = 0; k < 4; ++k) ml[k] = *(lds_f32x2_p)(uintptr_t)(lds0 + LDS_ML + (unsigned)((rg * 4 + k) * 32 + qb * 16 + e16) * 8u);
      const float M = fmaxf(fmaxf(ml[0][0], ml[1][0]), fmaxf(ml[2][0], ml[3][0]));
      float fk[4], L = 0.f;
#pragma unroll
      for (int k = 0; k < 4; ++k) { fk[k] = __builtin_amdgcn_exp2f(ml[k][0] - M); L += ml[k][1] * fk[k]; }
      const float inv = 1.0f / L;
      float acc[8];
#pragma unroll
      for (int e = 0; e < 4; ++e) { acc[e] = o[2 * KG][qb][e] * (fk[KG] * inv); acc[4 + e] = o[2 * KG + 1][qb][e] * (fk[KG] * inv); }
#pragma unroll
      for (int src = 0; src < 3; ++src) {
        const float c = fk[(KG + 1 + src) & 3] * inv;
#pragma unroll
        for (int e = 0; e < 4; ++e) {
          const unsigned x = w[2 * src + qb][e];
          acc[2 * e] += __uint_as_float(x << 16) * c; acc[2 * e + 1] += __uint_as_float(x & 0xffff0000u) * c;
        }
      }
      unsigned a0 = cvtpk(acc[0], acc[1]), a1 = cvtpk(acc[2], acc[3]), b0 = cvtpk(acc[4], acc[5]), b1 = cvtpk(acc[6], acc[7]);
      auto r0 = __builtin_amdgcn_permlane16_swap(a0, b0, false, false);
      auto r1 = __builtin_amdgcn_permlane16_swap(a1, b1, false, false);
      u32x4 wv = {r0[0], r1[0], r0[1], r1[1]};
      *(u32x4*)(op + qb * 16 * (D * 2)) = wv;
    }
  };
  if (kg == 0) fin(std::integral_constant<int, 0>{});
  else if (kg == 1) fin(std::integral_constant<int, 1>{});
  else if (kg == 2) fin(std::integral_constant<int, 2>{});
  else fin(std::integral_constant<int, 3>{});
}
}

extern "C" void kernel_launch(void* const* d_in, const int* in_sizes, int n_in,
                              void* d_out, int out_size, void* d_ws, size_t ws_size,
                              hipStream_t stream) {
  (void)in_sizes; (void)n_in; (void)out_size; (void)d_ws; (void)ws_size;
  (void)hipFuncSetAttribute((const void*)gqa_fwd, hipFuncAttributeMaxDynamicSharedMemorySize, LDS_BYTES);
  hipLaunchKernelGGL(gqa_fwd, dim3(NB * HQ), dim3(NTHR), LDS_BYTES, stream,
                     (const __hip_bfloat16*)d_in[0], (const __hip_bfloat16*)d_in[1], (const __hip_bfloat16*)d_in[2], (__hip_bfloat16*)d_out);
}
```

```cpp
#include <hip/hip_runtime.h>
#include <hip/hip_bf16.h>
#include <stdint.h>
#include <type_traits>

namespace {
constexpr int NB = 8, HQ = 32, HKV = 16, SQ = 96, SK = 2048, D = 128;
constexpr int NWAVES = 12, NTHR = NWAVES * 64;
constexpr int TK = 128, NT = SK / TK;
constexpr int SLOT = TK * D * 2;
constexpr int LDS_K = 0, LDS_V = 2 * SLOT;
constexpr int LDS_ML = 0;
constexpr int LDS_BYTES = 5 * SLOT;
constexpr float CSC = 0.088388347648318447f * 1.4426950408889634f;
constexpr float THR = 8.0f;

typedef float f32x16 __attribute__((ext_vector_type(16)));
typedef float f32x4 __attribute__((ext_vector_type(4)));
typedef float f32x2 __attribute__((ext_vector_type(2)));
typedef short s16x8 __attribute__((ext_vector_type(8)));
typedef short s16x4 __attribute__((ext_vector_type(4)));
typedef __bf16 bf16x2 __attribute__((ext_vector_type(2)));
typedef unsigned u32x4 __attribute__((ext_vector_type(4)));
typedef unsigned u32x2 __attribute__((ext_vector_type(2)));
typedef __attribute__((address_space(3))) s16x4* lds_s16x4_p;
typedef __attribute__((address_space(3))) s16x8* lds_s16x8_p;
typedef __attribute__((address_space(3))) f32x4* lds_f32x4_p;
typedef __attribute__((address_space(3))) f32x2* lds_f32x2_p;

#define DI __device__ __forceinline__
DI unsigned cvtpk(float lo, float hi) { f32x2 v = {lo, hi}; bf16x2 b = __builtin_convertvector(v, bf16x2); return __builtin_bit_cast(unsigned, b); }
DI float swap_max(float x) { auto r = __builtin_amdgcn_permlane32_swap(__float_as_uint(x), __float_as_uint(x), false, false); return fmaxf(__uint_as_float(r[0]), __uint_as_float(r[1])); }
DI float swap16_max(float x) { auto r = __builtin_amdgcn_permlane16_swap(__float_as_uint(x), __float_as_uint(x), false, false); return fmaxf(__uint_as_float(r[0]), __uint_as_float(r[1])); }
DI float swap16_add(float x) { auto r = __builtin_amdgcn_permlane16_swap(__float_as_uint(x), __float_as_uint(x), false, false); return __uint_as_float(r[0]) + __uint_as_float(r[1]); }
#define MFMA16(a, b, c) __builtin_amdgcn_mfma_f32_16x16x32_bf16((a), (b), (c), 0, 0, 0)
DI float swap_add(float x) { auto r = __builtin_amdgcn_permlane32_swap(__float_as_uint(x), __float_as_uint(x), false, false); return __uint_as_float(r[0]) + __uint_as_float(r[1]); }

DI void dma1(unsigned m0v, unsigned voff, const void* sbase) {
  asm volatile("s_mov_b32 m0, %0\n\ts_nop 0\n\tglobal_load_lds_dwordx4 %1, %2 nt" :: "s"(m0v), "v"(voff), "s"(sbase) : "memory");
}
DI void dma1_skip2(unsigned m0v, unsigned voff, const void* sbase, int sel) {
  asm volatile("s_cmp_eq_u32 %3, 2\n\ts_cbranch_scc1 1f\n\ts_mov_b32 m0, %0\n\ts_nop 0\n\tglobal_load_lds_dwordx4 %1, %2 nt\n1:"
               :: "s"(m0v), "v"(voff), "s"(sbase), "s"(sel) : "memory", "scc");
}
#define WAITVM(n) asm volatile("s_waitcnt vmcnt(" #n ")" ::: "memory")
#define WAITVM2(a, b, sel) asm volatile("s_cmp_eq_u32 %0, 2\n\ts_cbranch_scc1 1f\n\ts_waitcnt vmcnt(" #a ")\n\ts_branch 2f\n1:\ts_waitcnt vmcnt(" #b ")\n2:" :: "s"(sel) : "memory", "scc")
#define PIN(x) asm volatile("" : "+v"(x))
#define BARRIER() asm volatile("s_waitcnt lgkmcnt(0)\n\ts_barrier" ::: "memory")

__global__ __launch_bounds__(NTHR, 3)
void gqa_fwd(const __hip_bfloat16* __restrict__ Q, const __hip_bfloat16* __restrict__ K, const __hip_bfloat16* __restrict__ V,
             __hip_bfloat16* __restrict__ O) {
  extern __shared__ __attribute__((aligned(16))) char shm[];
  const unsigned lds0 = (unsigned)(uintptr_t)(__attribute__((address_space(3))) char*)shm;
  const int tid = threadIdx.x, lane = tid & 63;
  const int wid = __builtin_amdgcn_readfirstlane(tid >> 6);
  const int rg = wid >> 2, kg = wid & 3;
  const int c16 = lane & 15, g4 = lane >> 4;
  const int bid = blockIdx.x, xcd = bid & 7, slotid = bid >> 3;
  const int pair = xcd * 16 + (slotid >> 1), member = slotid & 1;
  const int bq = pair * 2 + member;
  const char* Kh = (const char*)K + (size_t)pair * SK * D * 2;
  const char* Vh = (const char*)V + (size_t)pair * SK * D * 2;

  const unsigned lrow = lane >> 4, lch = lane & 15;
  const unsigned ksrc = lrow * 256u + 16u * (lch ^ lrow);
  const unsigned vsrc = lrow * 256u + 16u * (lch ^ (lrow << 2));
  const unsigned pofs = kg * 8192u + rg * 1024u;
  auto pieceK = [&](int t, int k) {
      const unsigned d = lds0 + LDS_K + (t & 1) * SLOT + pofs + 3072u * k, v = ksrc ^ (64u * ((rg + 3 * k) & 3)); const char* g = Kh + (size_t)t * SLOT + pofs + 3072u * k;
      if (k < 2) dma1(d, v, g); else dma1_skip2(d, v, g, rg); };
  auto pieceV = [&](int t, unsigned vslot, int k) {
      const unsigned d = lds0 + LDS_V + vslot + pofs + 3072u * k; const char* g = Vh + (size_t)t * SLOT + pofs + 3072u * k;
      const unsigned v = vsrc ^ (32u * ((rg + 3 * k) & 1));
      if (k < 2) dma1(d, v, g); else dma1_skip2(d, v, g, rg); };
  {
    const char* qg = (const char*)Q + ((size_t)bq * SQ + rg * 32) * (D * 2) + kg * 2048;
    const unsigned qd = lds0 + LDS_V + 2 * SLOT + rg * 8192u + kg * 2048u;
    dma1(qd, ksrc ^ (64u * ((2 * kg) & 3)), qg);
    dma1(qd + 1024u, ksrc ^ (64u * ((2 * kg + 1) & 3)), qg + 1024);
  }
#pragma unroll
  for (int k = 0; k < 3; ++k) pieceK(0, k);
  asm volatile("s_barrier" ::: "memory");
#pragma unroll
  for (int k = 0; k < 3; ++k) pieceK(1, k);
#pragma unroll
  for (int k = 0; k < 3; ++k) pieceV(0, 0, k);
  s16x8 qf[8];

  const unsigned kaddr = lds0 + LDS_K + (kg * 32 + c16) * 256u + 16u * ((unsigned)g4 ^ (unsigned)c16);
  const unsigned tq = (lane >> 2) & 3, tp = lane & 3;
  const unsigned vaddr = lds0 + LDS_V + kg * 8192u + (4u * g4 + tq) * 256u + 16u * ((tp >> 1) ^ (tq << 2) ^ ((g4 & 1u) << 1)) + 8u * (tp & 1);

  f32x4 o[8][2];
#pragma unroll
  for (int i = 0; i < 8; ++i) { o[i][0] = f32x4{0.f, 0.f, 0.f, 0.f}; o[i][1] = f32x4{0.f, 0.f, 0.f, 0.f}; }
  float m0 = -__builtin_inff(), m1 = -__builtin_inff(), l0 = 0.f, l1 = 0.f;
  float mc = -__builtin_inff();

#define SB() __builtin_amdgcn_sched_barrier(0)
  struct S4 { f32x4 v[2][2]; };
  s16x8 kf[8];
  auto kread = [&](int i, unsigned ka) { kf[i] = *(lds_s16x8_p)(uintptr_t)((ka ^ (64u * (i >> 1))) + 4096u * (i & 1)); };
  auto body = [&](auto QKc, auto PVc, auto DVc, auto DKc, auto NXTc, const int t, const unsigned vsp, const unsigned vsn, S4& sp, S4& s) {
    constexpr bool QK = decltype(QKc)::value, PVV = decltype(PVc)::value, DV = decltype(DVc)::value, DK = decltype(DKc)::value, NXT = decltype(NXTc)::value;
    unsigned ka = kaddr + (t & 1) * SLOT, va0 = vaddr + vsp;
    asm volatile("" : "+v"(ka), "+v"(va0));
    u32x4 pw[2];
    const float nm[2] = {-m0, -m1};
    float ps[2] = {0.f, 0.f};
    auto expp = [&](int k) { f32x4& x = sp.v[(k >> 1) & 1][k >> 2]; const int r = 2 * (k & 1);
        float e0 = __builtin_amdgcn_exp2f(fmaf(x[r], CSC, nm[k >> 2])), e1 = __builtin_amdgcn_exp2f(fmaf(x[r + 1], CSC, nm[k >> 2]));
        PIN(e0); PIN(e1); x[r] = e0; x[r + 1] = e1; };
    auto packp = [&](int k) { f32x4& x = sp.v[(k >> 1) & 1][k >> 2]; const int r = 2 * (k & 1);
        unsigned w = cvtpk(x[r], x[r + 1]); float a = ps[k >> 2] + (x[r] + x[r + 1]); PIN(w); PIN(a); ps[k >> 2] = a; pw[k >> 2][k & 3] = w; };
    if constexpr (QK) {
#pragma unroll
      for (int i = 0; i < 4; ++i) s.v[i >> 1][i & 1] = f32x4{0.f, 0.f, 0.f, 0.f};
    }
    __builtin_amdgcn_s_setprio(1);
    SB();
#pragma unroll
    for (int g = 0; g < 8; ++g) {
      if (g == 6) __builtin_amdgcn_s_setprio(0);
      if constexpr (QK) {
        s.v[g & 1][0] = MFMA16(kf[g], qf[g & ~1], s.v[g & 1][0]);
        s.v[g & 1][1] = MFMA16(kf[g], qf[g | 1], s.v[g & 1][1]);
        if (g + 3 < 8) kread(g + 3, ka);
      }
      if constexpr (PVV) { if (g & 1) packp(g >> 1); else expp(g >> 1); }
      SB();
      if constexpr (DV) { if (g == 1 || g == 4 || g == 7) { pieceV(t, vsn, g / 3); SB(); } }
    }
    if constexpr (QK) {
      if constexpr (NXT) WAITVM2(3, 2, rg); else WAITVM(0);
      BARRIER();
    }
    __builtin_amdgcn_s_setprio(3);
    s16x8 vf[8];
    auto vread = [&](int db) {
      const unsigned va = va0 ^ (32u * db);
      const s16x4 lo = __builtin_amdgcn_ds_read_tr16_b64_v4i16((lds_s16x4_p)(uintptr_t)(va));
      const s16x4 up = __builtin_amdgcn_ds_read_tr16_b64_v4i16((lds_s16x4_p)(uintptr_t)(va + 4096u));
      vf[db] = __builtin_shufflevector(lo, up, 0, 1, 2, 3, 4, 5, 6, 7);
    };
    if constexpr (PVV) { vread(0); vread(1); vread(2); vread(3); expp(4); }
    SB();
    float mx0 = 0.f, mx1 = 0.f;
    if constexpr (QK) {
      mx0 = fmaxf(fmaxf(s.v[0][0][0], s.v[0][0][1]), s.v[0][0][2]); mx1 = fmaxf(fmaxf(s.v[0][1][0], s.v[0][1][1]), s.v[0][1][2]);
      mx0 = fmaxf(fmaxf(mx0, s.v[0][0][3]), s.v[1][0][0]); mx1 = fmaxf(fmaxf(mx1, s.v[0][1][3]), s.v[1][1][0]);
      mx0 = fmaxf(fmaxf(mx0, s.v[1][0][1]), s.v[1][0][2]); mx1 = fmaxf(fmaxf(mx1, s.v[1][1][1]), s.v[1][1][2]);
      mx0 = fmaxf(mx0, s.v[1][0][3]); mx1 = fmaxf(mx1, s.v[1][1][3]);
      PIN(mx0); PIN(mx1);
    }
    SB();
    unsigned kn = kaddr + ((t + 1) & 1) * SLOT;
    asm volatile("" : "+v"(kn));
#pragma unroll
    for (int g = 0; g < 8; ++g) {
      if (g == 6) __builtin_amdgcn_s_setprio(2);
      if constexpr (PVV) {
        const s16x8 p0 = __builtin_bit_cast(s16x8, pw[0]);
        if (g < 2) {
          o[2 * g][0] = MFMA16(vf[2 * g], p0, o[2 * g][0]); o[2 * g + 1][0] = MFMA16(vf[2 * g + 1], p0, o[2 * g + 1][0]);
          packp(4 + 2 * g); expp(5 + 2 * g); packp(5 + 2 * g); if (g == 0) expp(6);
        } else if (g < 4) {
          const s16x8 p1 = __builtin_bit_cast(s16x8, pw[1]);
          o[2 * g - 4][1] = MFMA16(vf[2 * g - 4], p1, o[2 * g - 4][1]); o[2 * g - 3][1] = MFMA16(vf[2 * g - 3], p1, o[2 * g - 3][1]);
          vread(g + 2 + (g - 2)); vread(g + 3 + (g - 2));
        } else {
          const s16x8 p1 = __builtin_bit_cast(s16x8, pw[1]);
          o[g][0] = MFMA16(vf[g], p0, o[g][0]); o[g][1] = MFMA16(vf[g], p1, o[g][1]);
        }
        if (g == 4) { l0 += ps[0]; l1 += ps[1]; PIN(l0); PIN(l1); }
      }
      if constexpr (QK) {
        if (g == 2) { auto r = __builtin_amdgcn_permlane32_swap(__float_as_uint(mx0), __float_as_uint(mx1), false, false);
                      mx0 = fmaxf(__uint_as_float(r[0]), __uint_as_float(r[1])); PIN(mx0); }
        if (g == 3) { mx0 = swap16_max(mx0) * CSC; PIN(mx0); }
      }
      if constexpr (QK && NXT) { if (g >= 5) kread(g - 5, kn); }
      SB();
      if constexpr (DK) { if (g == 0 || g == 3 || g == 6) { pieceK(t + 2, g / 3); SB(); } }
      if constexpr (QK && NXT && !DK) { if (g == 0 || g == 3 || g == 6) { pieceV(t + 1, ((NT - 1) % 3) * SLOT, g / 3); SB(); } }
    }
    if constexpr (QK) {
      if (__builtin_expect(__any(mx0 - mc > THR), 0)) {
        mc = fmaxf(mc, mx0);
        auto r = __builtin_amdgcn_permlane32_swap(__float_as_uint(mc), __float_as_uint(mc), false, false);
        const float mn0 = __uint_as_float(r[0]), mn1 = __uint_as_float(r[1]);
        const float a0 = __builtin_amdgcn_exp2f(m0 - mn0), a1 = __builtin_amdgcn_exp2f(m1 - mn1);
        m0 = mn0; m1 = mn1; l0 *= a0; l1 *= a1;
#pragma unroll
        for (int i = 0; i < 8; ++i) { o[i][0] *= a0; o[i][1] *= a1; }
      }
    }
  };
  {
    using T = std::true_type; using F = std::false_type;
    S4 sA, sB;
    WAITVM2(6, 4, rg);
    BARRIER();
    {
      const unsigned qa = lds0 + LDS_V + 2 * SLOT + rg * 8192u + c16 * 256u + 16u * ((unsigned)g4 ^ (unsigned)c16);
#pragma unroll
      for (int i = 0; i < 8; ++i) qf[i] = *(lds_s16x8_p)(uintptr_t)((qa ^ (64u * (i >> 1))) + 4096u * (i & 1));
      kread(0, kaddr); kread(1, kaddr); kread(2, kaddr);
    }
    body(T{}, F{}, F{}, T{}, T{}, 0, 0, 0, sB, sA);
    unsigned vsp = 0, vsn = SLOT;
#pragma nounroll
    for (int t = 1; t + 1 < NT - 2; t += 2) {
      body(T{}, T{}, T{}, T{}, T{}, t, vsp, vsn, sA, sB);
      vsp = vsn; vsn = vsn == 2 * SLOT ? 0 : vsn + SLOT;
      body(T{}, T{}, T{}, T{}, T{}, t + 1, vsp, vsn, sB, sA);
      vsp = vsn; vsn = vsn == 2 * SLOT ? 0 : vsn + SLOT;
    }
    body(T{}, T{}, T{}, T{}, T{}, NT - 3, ((NT - 4) % 3) * SLOT, ((NT - 3) % 3) * SLOT, sA, sB);
    body(T{}, T{}, T{}, F{}, T{}, NT - 2, ((NT - 3) % 3) * SLOT, ((NT - 2) % 3) * SLOT, sB, sA);
    body(T{}, T{}, F{}, F{}, F{}, NT - 1, ((NT - 2) % 3) * SLOT, 0, sA, sB);
    body(F{}, T{}, F{}, F{}, F{}, NT, ((NT - 1) % 3) * SLOT, 0, sB, sA);
  }

  __builtin_amdgcn_s_setprio(0);
  int lane2 = lane; asm volatile("" : "+v"(lane2));
  const int e16 = lane2 & 15, eg = lane2 >> 4;
  l0 = swap16_add(swap_add(l0)); l1 = swap16_add(swap_add(l1));
  if (lane2 < 32) { f32x2 ml = {eg ? m1 : m0, eg ? l1 : l0}; *(lds_f32x2_p)(uintptr_t)(lds0 + LDS_ML + (unsigned)((rg * 4 + kg) * 32 + lane2) * 8u) = ml; }
  auto xaddr = [&](unsigned blk) { return lds0 + 4096u + blk * 2048u + (blk >= 30u ? (unsigned)(LDS_V + SLOT - 4096 - 30 * 2048) : 0u) + lane2 * 16u; };
  auto fin = [&](auto KGc) {
    constexpr int KG = decltype(KGc)::value;
#pragma unroll
    for (int dk = 0; dk < 4; ++dk) {
      if (dk != KG) {
        const unsigned src = (unsigned)(KG - dk - 1) & 3u;
        const unsigned base = xaddr((unsigned)(rg * 4 + dk) * 3u + src);
#pragma unroll
        for (int qb = 0; qb < 2; ++qb) {
          const f32x4 x = o[2 * dk][qb], y = o[2 * dk + 1][qb];
          u32x4 w = {cvtpk(x[0], x[1]), cvtpk(x[2], x[3]), cvtpk(y[0], y[1]), cvtpk(y[2], y[3])};
          *(__attribute__((address_space(3))) u32x4*)(uintptr_t)(base + qb * 1024u) = w;
        }
      }
    }
    BARRIER();
    u32x4 w[6];
#pragma unroll
    for (int src = 0; src < 3; ++src) {
      const unsigned base = xaddr((unsigned)(rg * 4 + KG) * 3u + src);
      w[2 * src] = *(__attribute__((address_space(3))) u32x4*)(uintptr_t)(base);
      w[2 * src + 1] = *(__attribute__((address_space(3))) u32x4*)(uintptr_t)(base + 1024u);
    }
    char* op = (char*)O + ((size_t)bq * SQ + rg * 32 + e16) * (D * 2) + KG * 64 + (eg & 1) * 32 + (eg >> 1) * 16;
#pragma unroll
    for (int qb = 0; qb < 2; ++qb) {
      f32x2 ml[4];
#pragma unroll
      for (int k = 0; k < 4; ++k) ml[k] = *(lds_f32x2_p)(uintptr_t)(lds0 + LDS_ML + (unsigned)((rg * 4 + k) * 32 + qb * 16 + e16) * 8u);
      const float M = fmaxf(fmaxf(ml[0][0], ml[1][0]), fmaxf(ml[2][0], ml[3][0]));
      float fk[4], L = 0.f;
#pragma unroll
      for (int k = 0; k < 4; ++k) { fk[k] = __builtin_amdgcn_exp2f(ml[k][0] - M); L += ml[k][1] * fk[k]; }
      const float inv = 1.0f / L;
      float acc[8];
#pragma unroll
      for (int e = 0; e < 4; ++e) { acc[e] = o[2 * KG][qb][e] * (fk[KG] * inv); acc[4 + e] = o[2 * KG + 1][qb][e] * (fk[KG] * inv); }
#pragma unroll
      for (int src = 0; src < 3; ++src) {
        const float c = fk[(KG + 1 + src) & 3] * inv;
#pragma unroll
        for (int e = 0; e < 4; ++e) {
          const unsigned x = w[2 * src + qb][e];
          acc[2 * e] += __uint_as_float(x << 16) * c; acc[2 * e + 1] += __uint_as_float(x & 0xffff0000u) * c;
        }
      }
      unsigned a0 = cvtpk(acc[0], acc[1]), a1 = cvtpk(acc[2], acc[3]), b0 = cvtpk(acc[4], acc[5]), b1 = cvtpk(acc[6], acc[7]);
      auto r0 = __builtin_amdgcn_permlane16_swap(a0, b0, false, false);
      auto r1 = __builtin_amdgcn_permlane16_swap(a1, b1, false, false);
      u32x4 wv = {r0[0], r1[0], r0[1], r1[1]};
      *(u32x4*)(op + qb * 16 * (D * 2)) = wv;
    }
  };
  if (kg == 0) fin(std::integral_constant<int, 0>{});
  else if (kg == 1) fin(std::integral_constant<int, 1>{});
  else if (kg == 2) fin(std::integral_constant<int, 2>{});
  else fin(std::integral_constant<int, 3>{});
}
}

extern "C" void kernel_launch(void* const* d_in, const int* in_sizes, int n_in,
                              void* d_out, int out_size, void* d_ws, size_t ws_size,
                              hipStream_t stream) {
  (void)in_sizes; (void)n_in; (void)out_size; (void)d_ws; (void)ws_size;
  (void)hipFuncSetAttribute((const void*)gqa_fwd, hipFuncAttributeMaxDynamicSharedMemorySize, LDS_BYTES);
  hipLaunchKernelGGL(gqa_fwd, dim3(NB * HQ), dim3(NTHR), LDS_BYTES, stream,
                     (const __hip_bfloat16*)d_in[0], (const __hip_bfloat16*)d_in[1], (const __hip_bfloat16*)d_in[2], (__hip_bfloat16*)d_out);
}
```
